```python
import torch

def _softmax(x, axis=-1):
    return torch.softmax(x, dim=axis)
def _gelu(x):
    return torch.nn.functional.gelu(x, approximate="tanh")
def _silu(x):
    return torch.nn.functional.silu(x)
def _sigmoid(x):
    return torch.sigmoid(x)
def _layernorm(x, axis=-1, eps=1e-5):
    m = x.mean(dim=axis, keepdim=True)
    v = x.var(dim=axis, keepdim=True, unbiased=False)
    return (x - m) / torch.sqrt(v + eps)
def _scatter_add(tgt, idx, src, axis=0):
    return tgt.clone().index_add_(axis, idx, src)
def _segment_sum(src, seg, n):
    out = torch.zeros((n,) + src.shape[1:], dtype=src.dtype)
    return out.index_add_(0, seg, src)
def _pad(x, pw):
    flat = tuple(v for pair in reversed(pw) for v in pair)
    return torch.nn.functional.pad(x, flat)

kernel_name = 'concat_heads__grouped_kv_mqa__ssm_conv_s5__prompt'

def setup_inputs():

    _w = (lambda a: (lambda t: t.to(torch.bfloat16) if t.is_floating_point() else t)(torch.as_tensor(a)))
    def _A_setup():
        B, S, D, H = 1, 2048, 512, 32
        Dh = D // H
        return {
            "x": _w((lambda *s: torch.randn(*s, dtype=torch.bfloat16) * 0.05)(B, S, D)),
            "wq": _w((lambda *s: torch.randn(*s, dtype=torch.bfloat16) * 0.8)(D, H * Dh)),
            "wk": _w((lambda *s: torch.randn(*s, dtype=torch.bfloat16) * 0.8)(D, Dh)),
            "wv": _w((lambda *s: torch.randn(*s, dtype=torch.bfloat16) * 0.05)(D, Dh)),
            "wo": _w((lambda *s: torch.randn(*s, dtype=torch.bfloat16) * 0.05)(H * Dh, D)),
        }
    _A_in = _A_setup()

    def _B_setup():
        B, S, D = 1, 2048, 512
        P = D
        Lambda_re = -0.5 * (lambda _x: torch.exp(torch.as_tensor(_x)))((lambda *s: torch.randn(*s, dtype=torch.bfloat16) * 0.2)(P))
        Lambda_im = (lambda *s: torch.randn(*s, dtype=torch.bfloat16) * 0.4)(P) * 1.0
        B_re = (lambda *s: torch.randn(*s, dtype=torch.bfloat16) * 0.2)(P, D) * (D ** -0.5)
        B_im = (lambda *s: torch.randn(*s, dtype=torch.bfloat16) * 0.2)(P, D) * (D ** -0.5)
        C_re = (lambda *s: torch.randn(*s, dtype=torch.bfloat16) * 0.2)(D, P) * (P ** -0.5)
        C_im = (lambda *s: torch.randn(*s, dtype=torch.bfloat16) * 0.2)(D, P) * (P ** -0.5)
        log_dt = (lambda *s: torch.randn(*s, dtype=torch.bfloat16) * 0.2)(P) * 0.3 - 2.0
        Dskip = (lambda *s: torch.randn(*s, dtype=torch.bfloat16) * 0.05)(D)
        return {
            "x": _w((lambda *s: torch.randn(*s, dtype=torch.bfloat16) * 0.05)(B, S, D)),
            "Lambda_re": _w(Lambda_re),
            "Lambda_im": _w(Lambda_im),
            "B_re": _w(B_re),
            "B_im": _w(B_im),
            "C_re": _w(C_re),
            "C_im": _w(C_im),
            "log_dt": _w(log_dt),
            "Dskip": _w(Dskip),
        }
    _B_in = _B_setup()

    return {
        "A_x": _A_in["x"],
        "A_wq": _A_in["wq"],
        "A_wk": _A_in["wk"],
        "A_wv": _A_in["wv"],
        "A_wo": _A_in["wo"],
        "B_x": _B_in["x"],
        "B_Lambda_re": _B_in["Lambda_re"],
        "B_Lambda_im": _B_in["Lambda_im"],
        "B_B_re": _B_in["B_re"],
        "B_B_im": _B_in["B_im"],
        "B_C_re": _B_in["C_re"],
        "B_C_im": _B_in["C_im"],
        "B_log_dt": _B_in["log_dt"],
        "B_Dskip": _B_in["Dskip"],
    }

def reference(A_x, A_wq, A_wk, A_wv, A_wo, B_x, B_Lambda_re, B_Lambda_im, B_B_re, B_B_im, B_C_re, B_C_im, B_log_dt, B_Dskip):
    def _A_ref(x, wq, wk, wv, wo):
        B, S, D = x.shape
        H = 32
        Dh = D // H
        q = (lambda x, *d: x.permute(*d))((x @ wq).reshape(B, S, H, Dh), 0, 2, 1, 3)
        k = x @ wk
        v = x @ wv
        scores = torch.einsum("bhqd,bkd->bhqk", q, k) * (Dh ** -0.5)
        p = _softmax(scores, axis=-1)
        o = torch.einsum("bhqk,bkd->bhqd", p, v)
        return (lambda x, *d: x.permute(*d))(o, 0, 2, 1, 3).reshape(B, S, H * Dh) @ wo

    def _B_ref(x, Lambda_re, Lambda_im, B_re, B_im, C_re, C_im, log_dt, Dskip):
        Bsz, L, H = x.shape
        Lam = Lambda_re + 1j * Lambda_im
        Bmat = B_re + 1j * B_im
        Cmat = C_re + 1j * C_im
        dt = torch.exp(log_dt)
        Lam_bar = torch.exp(dt * Lam)
        B_bar = ((Lam_bar - 1.0) / Lam)[:, None] * Bmat
        Bu = torch.einsum("bld,pd->blp", x + 0j, B_bar)
        h = torch.zeros_like(Bu[:, 0, :])
        hs = []
        for t in range(L):
            h = Lam_bar[None, :] * h + Bu[:, t, :]
            hs.append(h)
        hseq = torch.stack(hs, 1)
        y = 2.0 * torch.einsum("blp,dp->bld", hseq, Cmat).real + x * Dskip[None, None, :]
        return y

    a = _A_ref(A_x, A_wq, A_wk, A_wv, A_wo)
    b = _B_ref(B_x, B_Lambda_re, B_Lambda_im, B_B_re, B_B_im, B_C_re, B_C_im, B_log_dt, B_Dskip)
    return torch.concatenate([a, b], axis=-1)
```

```cpp
#include <hip/hip_runtime.h>
#include <hip/hip_bf16.h>
typedef unsigned short u16;
typedef float f32x16 __attribute__((ext_vector_type(16)));
typedef float f32x8 __attribute__((ext_vector_type(8)));
typedef __bf16 bf16x8 __attribute__((ext_vector_type(8)));
typedef __bf16 bf16x16 __attribute__((ext_vector_type(16)));
__device__ __forceinline__ float bf2f(u16 v) { return __uint_as_float((unsigned)v << 16); }
__device__ __forceinline__ u16 f2bf(float f) { return __builtin_bit_cast(u16, (__bf16)f); }
#define EXTRA 0
__global__ void k_tr(const u16* w, u16* wt, int K, int N) {
  int i = blockIdx.x * 256 + threadIdx.x; if (i >= K * N) return;
  int n = i / K, k = i % K; wt[i] = w[k * N + n];
}
__device__ __forceinline__ f32x16 tile(const u16* A, int lda, const u16* B, int ldb, int K, f32x16 acc, int lane) {
  const u16* a = A + (lane & 31) * lda + (lane >> 5) * 8;
  const u16* b = B + (lane & 31) * ldb + (lane >> 5) * 8;
#pragma unroll 8
  for (int k = 0; k < K; k += 16) acc = __builtin_amdgcn_mfma_f32_32x32x16_bf16(*(const bf16x8*)(a + k), *(const bf16x8*)(b + k), acc, 0, 0, 0);
  return acc;
}
__global__ void __launch_bounds__(64) k_proj(const u16* xa, const u16* wt, u16* qkv, const u16* xb, const u16* Bre, const u16* Bim, float* U) {
  int lane = threadIdx.x, tt = blockIdx.x, ct = blockIdx.y;
  f32x16 acc = {0};
  if (ct < 17) {
    acc = tile(xa + tt * 32 * 512, 512, wt + ct * 32 * 512, 512, 512, acc, lane);
    float sc = ct < 16 ? 0.25f * 1.4426950408889634f : 1.f;
    for (int r = 0; r < 16; ++r) { int row = (r & 3) + 8 * (r >> 2) + 4 * (lane >> 5); qkv[(tt * 32 + row) * 544 + ct * 32 + (lane & 31)] = f2bf(acc[r] * sc); }
  } else {
    int c = ct - 17; const u16* B = c < 16 ? Bre + c * 32 * 512 : Bim + (c - 16) * 32 * 512;
    acc = tile(xb + tt * 32 * 512, 512, B, 512, 512, acc, lane);
    for (int r = 0; r < 16; ++r) { int row = (r & 3) + 8 * (r >> 2) + 4 * (lane >> 5); U[(tt * 32 + row) * 1024 + c * 32 + (lane & 31)] = acc[r]; }
  }
}
__global__ void __launch_bounds__(64) k_attn(const u16* qkv, u16* o) {
  int lane = threadIdx.x, tt = blockIdx.x, h = blockIdx.y, hh = lane >> 5, c = lane & 31;
  bf16x8 q = *(const bf16x8*)(qkv + (tt * 32 + c) * 544 + h * 16 + hh * 8);
  f32x16 O = {0};
  for (int kt = 0; kt < 64; ++kt) {
    bf16x8 k = *(const bf16x8*)(qkv + (kt * 32 + c) * 544 + 512 + hh * 8);
    f32x16 z = {0};
    f32x16 s = __builtin_amdgcn_mfma_f32_32x32x16_bf16(k, q, z, 0, 0, 0);
    for (int g = 0; g < 2; ++g) {
      bf16x8 v;
      for (int j = 0; j < 8; ++j) v[j] = c < 16 ? __builtin_bit_cast(__bf16, qkv[(kt * 32 + g * 16 + hh * 8 + j) * 544 + 528 + c]) : (__bf16)1.0f;
      f32x8 p; for (int j = 0; j < 8; ++j) p[j] = __builtin_amdgcn_exp2f(s[g * 8 + j]);
      O = __builtin_amdgcn_smfmac_f32_32x32x32_bf16(v, __builtin_bit_cast(bf16x16, p), O, 0xDDDD, 0, 0);
    }
  }
  float r = 1.f / O[8];
  for (int j = 0; j < 8; ++j) o[(tt * 32 + c) * 512 + h * 16 + (j & 3) + 8 * (j >> 2) + 4 * hh] = f2bf(O[j] * r);
}
__device__ void consts(const u16* Lre, const u16* Lim, const u16* ldt, int p, float& ar, float& ai, float& cr, float& ci) {
  float dt = bf2f(f2bf(expf(bf2f(ldt[p])))), lr = bf2f(Lre[p]), li = bf2f(Lim[p]);
  float e = expf(dt * lr), sn, cs; sincosf(dt * li, &sn, &cs);
  ar = e * cs; ai = e * sn;
  float d = 1.f / (lr * lr + li * li), nr = ar - 1.f, ni = ai;
  cr = (nr * lr + ni * li) * d; ci = (ni * lr - nr * li) * d;
}
__global__ void __launch_bounds__(512) k_scan1(const float* U, const u16* Lre, const u16* Lim, const u16* ldt, float2* E) {
  int p = threadIdx.x, c = blockIdx.x; float ar, ai, cr, ci; consts(Lre, Lim, ldt, p, ar, ai, cr, ci);
  float hr = 0, hi = 0;
  for (int t = 0; t < 32; ++t) { float ur = U[(c * 32 + t) * 1024 + p], ui = U[(c * 32 + t) * 1024 + 512 + p]; float nr = ar * hr - ai * hi + ur, ni = ar * hi + ai * hr + ui; hr = nr; hi = ni; }
  E[c * 512 + p] = make_float2(hr, hi);
}
__global__ void __launch_bounds__(512) k_scan2(const float* U, const u16* Lre, const u16* Lim, const u16* ldt, const float2* E, u16* H) {
  int p = threadIdx.x, c = blockIdx.x; float ar, ai, cr, ci; consts(Lre, Lim, ldt, p, ar, ai, cr, ci);
  float pr = ar, pi = ai; for (int i = 0; i < 5; ++i) { float nr = pr * pr - pi * pi, ni = 2 * pr * pi; pr = nr; pi = ni; }
  float hr = 0, hi = 0;
  for (int k = 0; k < c; ++k) { float2 e = E[k * 512 + p]; float nr = pr * hr - pi * hi + e.x, ni = pr * hi + pi * hr + e.y; hr = nr; hi = ni; }
  for (int t = 0; t < 32; ++t) { float ur = U[(c * 32 + t) * 1024 + p], ui = U[(c * 32 + t) * 1024 + 512 + p]; float nr = ar * hr - ai * hi + ur, ni = ar * hi + ai * hr + ui; hr = nr; hi = ni;
    H[(c * 32 + t) * 1024 + p] = f2bf(2.f * (cr * hr - ci * hi)); H[(c * 32 + t) * 1024 + 512 + p] = f2bf(-2.f * (cr * hi + ci * hr)); }
}
__global__ void __launch_bounds__(64) k_out(const u16* o, const u16* wot, const u16* H, const u16* Cre, const u16* Cim, const u16* xb, const u16* Dk, float* out) {
  int lane = threadIdx.x, tt = blockIdx.x, ct = blockIdx.y;
  f32x16 acc = {0};
  if (ct < 16) acc = tile(o + tt * 32 * 512, 512, wot + ct * 32 * 512, 512, 512, acc, lane);
  else { int c = ct - 16; acc = tile(H + tt * 32 * 1024, 1024, Cre + c * 32 * 512, 512, 512, acc, lane); acc = tile(H + tt * 32 * 1024 + 512, 1024, Cim + c * 32 * 512, 512, 512, acc, lane); }
  for (int r = 0; r < 16; ++r) { int row = tt * 32 + (r & 3) + 8 * (r >> 2) + 4 * (lane >> 5), col = ct * 32 + (lane & 31);
    float v = acc[r]; if (ct >= 16) v += bf2f(xb[row * 512 + col - 512]) * bf2f(Dk[col - 512]); out[row * 1024 + col] = v; }
}
__global__ void k_nop(float* p) { if (threadIdx.x == 0) p[blockIdx.x] = 0.f; }
extern "C" void kernel_launch(void* const* d_in, const int* in_sizes, int n_in, void* d_out, int out_size, void* d_ws, size_t ws_size, hipStream_t stream) {
  const u16* const* in = (const u16* const*)d_in;
  char* ws = (char*)d_ws;
  u16* wt = (u16*)ws; u16* wot = (u16*)(ws + (1 << 20)); u16* qkv = (u16*)(ws + (2 << 20)); u16* o = (u16*)(ws + (6 << 20));
  float* U = (float*)(ws + (8 << 20)); u16* H = (u16*)(ws + (16 << 20)); float2* E = (float2*)(ws + (20 << 20));
  k_tr<<<1024, 256, 0, stream>>>(in[1], wt, 512, 512);
  k_tr<<<32, 256, 0, stream>>>(in[2], wt + 512 * 512, 512, 16);
  k_tr<<<32, 256, 0, stream>>>(in[3], wt + 528 * 512, 512, 16);
  k_tr<<<1024, 256, 0, stream>>>(in[4], wot, 512, 512);
  k_proj<<<dim3(64, 49), 64, 0, stream>>>(in[0], wt, qkv, in[5], in[8], in[9], U);
  k_attn<<<dim3(64, 32), 64, 0, stream>>>(qkv, o);
  k_scan1<<<64, 512, 0, stream>>>(U, in[6], in[7], in[12], E);
  k_scan2<<<64, 512, 0, stream>>>(U, in[6], in[7], in[12], E, H);
  k_out<<<dim3(64, 32), 64, 0, stream>>>(o, wot, H, in[10], in[11], in[5], in[13], (float*)d_out);
  for (int i = 0; i < EXTRA; ++i) k_nop<<<256, 64, 0, stream>>>((float*)(ws + (24 << 20)));
}
```

```cpp
#include <hip/hip_runtime.h>
#include <hip/hip_bf16.h>
typedef unsigned short u16;
typedef float f32x16 __attribute__((ext_vector_type(16)));
typedef float f32x8 __attribute__((ext_vector_type(8)));
typedef __bf16 bf16x8 __attribute__((ext_vector_type(8)));
typedef __bf16 bf16x16 __attribute__((ext_vector_type(16)));
__device__ __forceinline__ float bf2f(u16 v) { return __uint_as_float((unsigned)v << 16); }
__device__ __forceinline__ u16 f2bf(float f) { return __builtin_bit_cast(u16, (__bf16)f); }
#define EXTRA 0
__global__ void k_tr(const u16* w, u16* wt, int K, int N) {
  int i = blockIdx.x * 256 + threadIdx.x; if (i >= K * N) return;
  int n = i / K, k = i % K; wt[i] = w[k * N + n];
}
__device__ __forceinline__ f32x16 tile(const u16* A, int lda, const u16* B, int ldb, int K, f32x16 acc, int lane) {
  const u16* a = A + (lane & 31) * lda + (lane >> 5) * 8;
  const u16* b = B + (lane & 31) * ldb + (lane >> 5) * 8;
#pragma unroll 8
  for (int k = 0; k < K; k += 16) acc = __builtin_amdgcn_mfma_f32_32x32x16_bf16(*(const bf16x8*)(a + k), *(const bf16x8*)(b + k), acc, 0, 0, 0);
  return acc;
}
__global__ void __launch_bounds__(64) k_proj(const u16* xa, const u16* wt, u16* qkv, const u16* xb, const u16* Bre, const u16* Bim, float* U) {
  int lane = threadIdx.x, tt = blockIdx.x, ct = blockIdx.y;
  f32x16 acc = {0};
  if (ct < 17) {
    acc = tile(xa + tt * 32 * 512, 512, wt + ct * 32 * 512, 512, 512, acc, lane);
    float sc = ct < 16 ? 0.25f * 1.4426950408889634f : 1.f;
    for (int r = 0; r < 16; ++r) { int row = (r & 3) + 8 * (r >> 2) + 4 * (lane >> 5); qkv[(tt * 32 + row) * 544 + ct * 32 + (lane & 31)] = f2bf(acc[r] * sc); }
  } else {
    int c = ct - 17; const u16* B = c < 16 ? Bre + c * 32 * 512 : Bim + (c - 16) * 32 * 512;
    acc = tile(xb + tt * 32 * 512, 512, B, 512, 512, acc, lane);
    for (int r = 0; r < 16; ++r) { int row = (r & 3) + 8 * (r >> 2) + 4 * (lane >> 5); U[(tt * 32 + row) * 1024 + c * 32 + (lane & 31)] = acc[r]; }
  }
}
__global__ void __launch_bounds__(64) k_attn(const u16* qkv, u16* o) {
  int lane = threadIdx.x, tt = blockIdx.x, h = blockIdx.y, hh = lane >> 5, c = lane & 31;
  bf16x8 q = *(const bf16x8*)(qkv + (tt * 32 + c) * 544 + h * 16 + hh * 8);
  f32x16 O = {0};
  for (int kt = 0; kt < 64; ++kt) {
    bf16x8 k = *(const bf16x8*)(qkv + (kt * 32 + c) * 544 + 512 + hh * 8);
    f32x16 z = {0};
    f32x16 s = __builtin_amdgcn_mfma_f32_32x32x16_bf16(k, q, z, 0, 0, 0);
    for (int g = 0; g < 2; ++g) {
      bf16x8 v;
      for (int j = 0; j < 8; ++j) v[j] = c < 16 ? __builtin_bit_cast(__bf16, qkv[(kt * 32 + g * 16 + hh * 8 + j) * 544 + 528 + c]) : (__bf16)1.0f;
      f32x8 p; for (int j = 0; j < 8; ++j) p[j] = __builtin_amdgcn_exp2f(s[g * 8 + j]);
      O = __builtin_amdgcn_smfmac_f32_32x32x32_bf16(v, __builtin_bit_cast(bf16x16, p), O, 0xDDDD, 0, 0);
    }
  }
  float r = 1.f / O[8];
  for (int j = 0; j < 8; ++j) o[(tt * 32 + c) * 512 + h * 16 + (j & 3) + 8 * (j >> 2) + 4 * hh] = f2bf(O[j] * r);
}
__device__ void consts(const u16* Lre, const u16* Lim, const u16* ldt, int p, float& ar, float& ai, float& cr, float& ci) {
  float dt = bf2f(f2bf(expf(bf2f(ldt[p])))), lr = bf2f(Lre[p]), li = bf2f(Lim[p]);
  float e = expf(dt * lr), sn, cs; sincosf(dt * li, &sn, &cs);
  ar = e * cs; ai = e * sn;
  float d = 1.f / (lr * lr + li * li), nr = ar - 1.f, ni = ai;
  cr = (nr * lr + ni * li) * d; ci = (ni * lr - nr * li) * d;
}
__global__ void __launch_bounds__(512) k_scan1(const float* U, const u16* Lre, const u16* Lim, const u16* ldt, float2* E) {
  int p = threadIdx.x, c = blockIdx.x; float ar, ai, cr, ci; consts(Lre, Lim, ldt, p, ar, ai, cr, ci);
  float hr = 0, hi = 0;
  for (int t = 0; t < 32; ++t) { float ur = U[(c * 32 + t) * 1024 + p], ui = U[(c * 32 + t) * 1024 + 512 + p]; float nr = ar * hr - ai * hi + ur, ni = ar * hi + ai * hr + ui; hr = nr; hi = ni; }
  E[c * 512 + p] = make_float2(hr, hi);
}
__global__ void __launch_bounds__(512) k_scan2(const float* U, const u16* Lre, const u16* Lim, const u16* ldt, const float2* E, u16* H) {
  int p = threadIdx.x, c = blockIdx.x; float ar, ai, cr, ci; consts(Lre, Lim, ldt, p, ar, ai, cr, ci);
  float pr = ar, pi = ai; for (int i = 0; i < 5; ++i) { float nr = pr * pr - pi * pi, ni = 2 * pr * pi; pr = nr; pi = ni; }
  float hr = 0, hi = 0;
  for (int k = 0; k < c; ++k) { float2 e = E[k * 512 + p]; float nr = pr * hr - pi * hi + e.x, ni = pr * hi + pi * hr + e.y; hr = nr; hi = ni; }
  for (int t = 0; t < 32; ++t) { float ur = U[(c * 32 + t) * 1024 + p], ui = U[(c * 32 + t) * 1024 + 512 + p]; float nr = ar * hr - ai * hi + ur, ni = ar * hi + ai * hr + ui; hr = nr; hi = ni;
    H[(c * 32 + t) * 1024 + p] = f2bf(2.f * (cr * hr - ci * hi)); H[(c * 32 + t) * 1024 + 512 + p] = f2bf(-2.f * (cr * hi + ci * hr)); }
}
__global__ void __launch_bounds__(64) k_out(const u16* o, const u16* wot, const u16* H, const u16* Cre, const u16* Cim, const u16* xb, const u16* Dk, float* out) {
  int lane = threadIdx.x, tt = blockIdx.x, ct = blockIdx.y;
  f32x16 acc = {0};
  if (ct < 16) acc = tile(o + tt * 32 * 512, 512, wot + ct * 32 * 512, 512, 512, acc, lane);
  else { int c = ct - 16; acc = tile(H + tt * 32 * 1024, 1024, Cre + c * 32 * 512, 512, 512, acc, lane); acc = tile(H + tt * 32 * 1024 + 512, 1024, Cim + c * 32 * 512, 512, 512, acc, lane); }
  for (int r = 0; r < 16; ++r) { int row = tt * 32 + (r & 3) + 8 * (r >> 2) + 4 * (lane >> 5), col = ct * 32 + (lane & 31);
    float v = acc[r]; if (ct >= 16) v += bf2f(xb[row * 512 + col - 512]) * bf2f(Dk[col - 512]); out[row * 1024 + col] = v; }
}
__global__ void __launch_bounds__(768) k_first(unsigned long long* p) {
  __shared__ char sm[16];
  unsigned long long t = __builtin_amdgcn_s_memrealtime();
  if (threadIdx.x == 0) { sm[0] = 1; p[blockIdx.x] = t + (((volatile char*)sm)[0] - 1); }
}
__global__ void __launch_bounds__(768) k_last(const unsigned long long* p, float* q) {
  __shared__ char sm[16];
  unsigned long long t0 = p[0];
  for (int i = 0; i < 1000000 && __builtin_amdgcn_s_memrealtime() < t0 + 30000; ++i) __builtin_amdgcn_s_sleep(1);
  if (threadIdx.x == 0) { sm[0] = 1; q[blockIdx.x] = ((volatile char*)sm)[0]; }
}
__global__ void k_nop(float* p) { if (threadIdx.x == 0) p[blockIdx.x] = 0.f; }
extern "C" void kernel_launch(void* const* d_in, const int* in_sizes, int n_in, void* d_out, int out_size, void* d_ws, size_t ws_size, hipStream_t stream) {
  const u16* const* in = (const u16* const*)d_in;
  char* ws = (char*)d_ws;
  u16* wt = (u16*)ws; u16* wot = (u16*)(ws + (1 << 20)); u16* qkv = (u16*)(ws + (2 << 20)); u16* o = (u16*)(ws + (6 << 20));
  float* U = (float*)(ws + (8 << 20)); u16* H = (u16*)(ws + (16 << 20)); float2* E = (float2*)(ws + (20 << 20));
  k_first<<<1, 64, 0, stream>>>((unsigned long long*)(ws + (25 << 20)));
  k_tr<<<1024, 256, 0, stream>>>(in[1], wt, 512, 512);
  k_tr<<<32, 256, 0, stream>>>(in[2], wt + 512 * 512, 512, 16);
  k_tr<<<32, 256, 0, stream>>>(in[3], wt + 528 * 512, 512, 16);
  k_tr<<<1024, 256, 0, stream>>>(in[4], wot, 512, 512);
  k_proj<<<dim3(64, 49), 64, 0, stream>>>(in[0], wt, qkv, in[5], in[8], in[9], U);
  k_attn<<<dim3(64, 32), 64, 0, stream>>>(qkv, o);
  k_scan1<<<64, 512, 0, stream>>>(U, in[6], in[7], in[12], E);
  k_scan2<<<64, 512, 0, stream>>>(U, in[6], in[7], in[12], E, H);
  k_out<<<dim3(64, 32), 64, 0, stream>>>(o, wot, H, in[10], in[11], in[5], in[13], (float*)d_out);
  k_last<<<1, 64, 0, stream>>>((unsigned long long*)(ws + (25 << 20)), (float*)(ws + (26 << 20)));
  for (int i = 0; i < EXTRA; ++i) k_nop<<<256, 64, 0, stream>>>((float*)(ws + (24 << 20)));
}
```

```cpp
#include <hip/hip_runtime.h>
#include <hip/hip_bf16.h>

typedef unsigned short u16;
typedef unsigned long long u64;
typedef float f32x2 __attribute__((ext_vector_type(2)));
typedef float f32x4 __attribute__((ext_vector_type(4)));
typedef float f32x8 __attribute__((ext_vector_type(8)));
typedef float f32x16 __attribute__((ext_vector_type(16)));
typedef __bf16 bf16x2 __attribute__((ext_vector_type(2)));
typedef __bf16 bf16x8 __attribute__((ext_vector_type(8)));
typedef __bf16 bf16x16 __attribute__((ext_vector_type(16)));
typedef short s16x4 __attribute__((ext_vector_type(4)));
typedef unsigned u32x2 __attribute__((ext_vector_type(2)));
typedef unsigned u32x4 __attribute__((ext_vector_type(4)));
typedef unsigned u32x8 __attribute__((ext_vector_type(8)));
typedef unsigned u32x16 __attribute__((ext_vector_type(16)));
typedef __amdgpu_buffer_rsrc_t rsrc_t;
#define LAS __attribute__((address_space(3)))
#define DEV __device__ __forceinline__

#define DBG 0

constexpr unsigned L_ONES = 0, L_CONST = 16, L_ZERO = 32, L_TAG = 48, L_GO = 96, L_PROG = 64, L_SB = 112;
constexpr unsigned L_SEEN = 116, L_QRDY = 120, L_LANDS = 124, L_KVP = 128, L_WO = 132, L_HS = 136, L_OD = 140, L_AB = 144, L_PB = 148, L_CR = 152, L_FIN = 160, L_XQ = 208, L_MID = 224, L_FL = 256, L_FS = 384, L_FX = 400, L_WL = 156, L_CST = 21504;
constexpr unsigned L_Q = 1024, OSLOT = 2304, L_OB = 9216, L_OA = 27648, L_R1 = 46080, L_R0 = 98304, L_WQ = 32768, L_WKV = 9216, L_KVS = 27648;
constexpr unsigned L_TOTAL = 163840;
constexpr unsigned W_KVF = 0x1000, W_HFL = 0x5000, W_NC = 0x8000, W_E = 0x10000, W_KV = 0x40000, W_O = 0x100000, W_H = 0x300000, W_END = 0x700000;
constexpr unsigned W_DBG = 0x800000;
constexpr unsigned SENT = 0xFFFFFFFFu;
constexpr int CAP = 200000;
constexpr int SC1 = 16;

struct Desc { const void* base; unsigned bytes, flags; };
struct Params {
  Desc xa, wq, ws, out, wk, wv;
  const void *lre, *lim, *ldt, *dsk, *wo, *xb, *bre, *bim, *cre, *cim;
};

DEV rsrc_t mk(const void* p, unsigned bytes) { return __builtin_amdgcn_make_buffer_rsrc((void*)p, 0, bytes, 0x00020000); }
#define PIN(x) asm volatile("" : "+v"(x))
struct Sync { unsigned zero, one; };
template <unsigned OFF> DEV void arrive(Sync y) {
  asm volatile("s_mov_b64 exec, 1\n\tds_add_u32 %0, %1 offset:%2\n\ts_mov_b64 exec, -1" ::"v"(y.zero), "v"(y.one), "i"(OFF) : "memory");
}
template <unsigned OFF, int SLEEP> DEV void await(unsigned base, unsigned target) {
  unsigned v, n;
  asm volatile("s_mov_b32 %1, %6\n"
               "Lw%=:\n\tds_read_b32 %0, %2 offset:%4\n\ts_waitcnt lgkmcnt(0)\n\tv_cmp_gt_u32 vcc, %3, %0\n\ts_sub_u32 %1, %1, 1\n\t"
               "s_cbranch_vccz Ld%=\n\ts_sleep %5\n\ts_cbranch_scc0 Lw%=\nLd%=:"
               : "=&v"(v), "=&s"(n) : "v"(base), "s"(target), "i"(OFF), "i"(SLEEP), "i"(CAP) : "memory", "vcc", "scc");
}
DEV bf16x8 rd128(unsigned a) { return *(LAS bf16x8*)a; }
DEV unsigned pk(float a, float b) { f32x2 v = {a, b}; return __builtin_bit_cast(unsigned, __builtin_convertvector(v, bf16x2)); }
DEV unsigned san(unsigned v) { return v < 0xFFFFFFFEu ? v : 0xFFFFFFFEu; }
DEV float bf2f(unsigned v) { return __uint_as_float(v << 16); }
DEV f32x16 mfma32(bf16x8 a, bf16x8 b, f32x16 c) { return __builtin_amdgcn_mfma_f32_32x32x16_bf16(a, b, c, 0, 0, 0); }
DEV f32x2 cmul(f32x2 a, f32x2 b) { return f32x2{a.x * b.x - a.y * b.y, a.x * b.y + a.y * b.x}; }
DEV void exact_tile(rsrc_t rws, unsigned q, unsigned dst) {
  unsigned lane;
  asm volatile("v_mbcnt_lo_u32_b32 %0, -1, 0\n\tv_mbcnt_hi_u32_b32 %0, -1, %0" : "=v"(lane));
  const unsigned vv = ((lane >> 5) * 16 + (lane & 15)) * 16;
  const bf16x8 qf = rd128(L_Q + q * 1024 + lane * 16);
  f32x16 O = {};
  float mrun = -__builtin_inff();
#pragma unroll 1
  for (unsigned kt = 0; kt < 64; ++kt) {
    const bf16x8 kf = __builtin_bit_cast(bf16x8, __builtin_amdgcn_raw_buffer_load_b128(rws, lane * 16, W_KV + kt * 2048, SC1));
    u32x4 va = __builtin_amdgcn_raw_buffer_load_b128(rws, vv, W_KV + kt * 2048 + 1024, SC1);
    u32x4 vb = __builtin_amdgcn_raw_buffer_load_b128(rws, vv, W_KV + kt * 2048 + 1536, SC1);
    if (lane & 16) va = vb = u32x4{0x3F803F80u, 0x3F803F80u, 0x3F803F80u, 0x3F803F80u};
    const f32x16 S = mfma32(kf, qf, f32x16{});
    float mt = S[0];
#pragma unroll
    for (unsigned e = 1; e < 16; ++e) mt = fmaxf(mt, S[e]);
    mt = fmaxf(mrun, fmaxf(mt, __int_as_float(__builtin_amdgcn_ds_bpermute((lane ^ 32) * 4, __float_as_int(mt)))));
    O *= (mt == mrun) ? 1.f : __builtin_amdgcn_exp2f(mrun - mt);
    mrun = mt;
    f32x8 pa, pb;
#pragma unroll
    for (unsigned e = 0; e < 8; ++e) { pa[e] = __builtin_amdgcn_exp2f(S[e] - mt); pb[e] = __builtin_amdgcn_exp2f(S[8 + e] - mt); }
    O = __builtin_amdgcn_smfmac_f32_32x32x32_bf16(__builtin_bit_cast(bf16x8, va), __builtin_bit_cast(bf16x16, pa), O, 0xDDDD, 0, 0);
    O = __builtin_amdgcn_smfmac_f32_32x32x32_bf16(__builtin_bit_cast(bf16x8, vb), __builtin_bit_cast(bf16x16, pb), O, 0xDDDD, 0, 0);
  }
  O *= 1.f / O[8];
  __builtin_amdgcn_raw_buffer_store_b128(u32x4{san(pk(O[0], O[1])), san(pk(O[2], O[3])), san(pk(O[4], O[5])), san(pk(O[6], O[7]))}, rws, lane * 16, dst, SC1);
}

#define LANE_VALUES                                                                                   \
  unsigned lane;                                                                                      \
  asm volatile("v_mbcnt_lo_u32_b32 %0, -1, 0\n\tv_mbcnt_hi_u32_b32 %0, -1, %0" : "=v"(lane));         \
  __builtin_assume(lane < 64);                                                                        \
  const unsigned i4 = lane >> 4, c16 = lane & 15, h = lane >> 5, r32 = lane & 31;                     \
  unsigned vw = (lane >> 3) * 1024 + (((lane & 7) ^ ((lane >> 2) & 4)) << 4);                         \
  Sync y = {0u, 1u};                                                                                  \
  PIN(y.zero); PIN(y.one);

__global__ void __launch_bounds__(768, 3) fused_kernel(Params) {
  __shared__ __attribute__((aligned(16))) char smem[L_TOTAL];
  const __attribute__((address_space(4))) Params* ka = (const __attribute__((address_space(4))) Params*)__builtin_amdgcn_kernarg_segment_ptr();
  const unsigned b = blockIdx.x;
  unsigned tid = threadIdx.x, wave, bad, raw_lr, raw_li, raw_dt, raw_dk;
  asm volatile(
      "s_load_dwordx16 s[8:23], %[ka], 0\n"
      "s_lshl_b32 s36, %[bid], 1\n"
      "s_and_b32 s36, s36, 16\n"
      "s_or_b32 s36, s36, 64\n"
      "s_load_dwordx4 s[24:27], %[ka], s36\n"
      "v_readfirstlane_b32 s60, %[tid]\n"
      "v_and_b32 v1, 63, %[tid]\n"
      "s_getpc_b64 s[88:89]\n"
      "v_mov_b32 v158, 0\n"
      "s_lshr_b32 s60, s60, 6\n"
      "s_setprio 3\n"
      "s_bitcmp1_b32 s60, 2\n"
      "s_cbranch_scc1 LB0%=\n"
      "s_and_b32 s61, s60, 3\n"
      "s_bfe_u32 s62, %[bid], 0x30003\n"
      "s_and_b32 s63, %[bid], 7\n"
      "s_lshr_b32 s42, %[bid], 6\n"
      "s_lshl2_add_u32 s63, s63, s42\n"
      "s_and_b32 s64, %[bid], 3\n"
      "s_lshr_b32 s65, s62, 1\n"
      "v_lshlrev_b32 v2, 4, v1\n"
      "v_lshrrev_b32 v9, 4, v1\n"
      "s_lshl_b32 s42, s61, 2\n"
      "v_bitop3_b32 v8, v9, v1, s42 bitop3:0x96\n"
      "v_and_b32 v8, 15, v8\n"
      "v_lshlrev_b32 v8, 4, v8\n"
      "v_lshl_or_b32 v8, v9, 10, v8\n"
      "s_lshl_b32 s52, s63, 16\n"
      "s_lshl_b32 s79, s61, 12\n"
      "s_add_u32 s52, s52, s79\n"
      "s_lshl_b32 s53, s61, 10\n"
      "s_cmp_ge_u32 s60, 4\n"
      "s_cbranch_scc1 LA0%=\n"
      "s_lshl_b32 s45, s65, 12\n"
      "s_lshl2_add_u32 s43, s45, s52\n"
      "s_add_u32 s45, s45, 98304\n"
      "s_add_u32 s47, s45, s53\n"
      "s_add_u32 s46, s53, 9216\n"
      "s_cmp_lg_u32 s61, 0\n"
      "s_cbranch_scc1 LS0%=\n"
      "s_lshl2_add_u32 s42, s63, s65\n"
      "s_lshr_b32 s50, s42, 1\n"
      "s_lshl_b32 s50, s50, 11\n"
      "s_and_b32 s48, s42, 1\n"
      "s_lshl_b32 s48, s48, 8\n"
      "s_lshl1_add_u32 s49, s48, 0x400\n"
      "s_bitcmp1_b32 s62, 0\n"
      "s_cselect_b32 s48, s49, s48\n"
      "s_cselect_b32 s49, 0, 0x100\n"
      "s_add_u32 s71, s50, s48\n"
      "s_add_u32 s71, s71, 262144\n"
      "v_lshrrev_b32 v13, 5, v1\n"
      "v_lshlrev_b32 v14, 3, v1\n"
      "v_mad_u32_u24 v12, v13, s49, v14\n"
      "s_movk_i32 s49, 0xf80\n"
      "v_mad_u32_u24 v13, v9, s49, v14\n"
      "v_mov_b64 v[14:15], -1\n"
      "s_lshl_b32 s49, s62, 14\n"
      "s_lshl_b32 s50, s63, 7\n"
      "s_sub_u32 s49, s49, s50\n"
      "s_add_u32 s49, s49, 69504\n"
      "s_waitcnt lgkmcnt(0)\n"
      "buffer_load_dwordx2 v[16:17], off, s[16:19], 0 sc1\n"
      "buffer_store_dwordx2 v[14:15], v12, s[16:19], s71 offen sc1\n"
      "buffer_store_dwordx2 v[14:15], v13, s[16:19], s49 offen sc1\n"
      "LS0%=:\n"
      "s_add_u32 m0, s47, 0\n"
      "s_lshl_b32 s42, s64, 8\n"
      "s_add_u32 s44, s42, s43\n"
      "s_waitcnt lgkmcnt(0)\n"
      "buffer_load_dwordx4 v8, s[8:11], s44 offen lds\n"
      "s_add_u32 m0, s46, 0\n"
      "s_lshl4_add_u32 s44, s42, s53\n"
      "buffer_load_dwordx4 v2, s[24:27], s44 offen lds\n"
      "s_add_u32 s41, s64, 1\n"
      "s_and_b32 s41, s41, 3\n"
      "s_add_u32 m0, s47, 16384\n"
      "s_lshl_b32 s42, s41, 8\n"
      "s_add_u32 s44, s42, s43\n"
      "buffer_load_dwordx4 v8, s[8:11], s44 offen lds\n"
      "s_add_u32 m0, s46, 4096\n"
      "s_lshl4_add_u32 s44, s42, s53\n"
      "buffer_load_dwordx4 v2, s[24:27], s44 offen lds\n"
      "s_add_u32 s41, s64, 2\n"
      "s_and_b32 s41, s41, 3\n"
      "s_add_u32 m0, s47, 32768\n"
      "s_lshl_b32 s42, s41, 8\n"
      "s_add_u32 s44, s42, s43\n"
      "buffer_load_dwordx4 v8, s[8:11], s44 offen lds\n"
      "s_add_u32 m0, s46, 8192\n"
      "s_lshl4_add_u32 s44, s42, s53\n"
      "buffer_load_dwordx4 v2, s[24:27], s44 offen lds\n"
      "s_add_u32 s41, s64, 3\n"
      "s_and_b32 s41, s41, 3\n"
      "s_add_u32 m0, s47, 49152\n"
      "s_lshl_b32 s42, s41, 8\n"
      "s_add_u32 s44, s42, s43\n"
      "buffer_load_dwordx4 v8, s[8:11], s44 offen lds\n"
      "s_add_u32 m0, s46, 12288\n"
      "s_lshl4_add_u32 s44, s42, s53\n"
      "buffer_load_dwordx4 v2, s[24:27], s44 offen lds\n"
      "s_load_dwordx8 s[28:35], %[ka], 0x60\n"
      "s_barrier\n"
      "s_lshl2_add_u32 m0, s61, 384\n"
      "s_mov_b64 exec, 1\n"
      "global_load_lds_dword v158, s[88:89]\n"
      "s_mov_b64 exec, -1\n"
      "s_branch LS2%=\n"
      "LB0%=:\n"
      "v_lshlrev_b32 v163, 2, v1\n"
      "ds_write2st64_b32 v163, v158, v158 offset1:1\n"
      "v_lshlrev_b32 v8, 7, %[tid]\n"
      "v_add_u32 v8, 0xffff8000, v8\n"
      "s_mov_b32 m0, 0x200\n"
      "v_min_u32 v8, 23040, v8\n"
      "global_load_lds_dword v8, s[88:89]\n"
      "s_and_b32 s61, s60, 3\n"
      "s_bfe_u32 s62, %[bid], 0x30003\n"
      "s_and_b32 s63, %[bid], 7\n"
      "s_lshr_b32 s42, %[bid], 6\n"
      "s_lshl2_add_u32 s63, s63, s42\n"
      "s_and_b32 s64, %[bid], 3\n"
      "s_lshr_b32 s65, s62, 1\n"
      "v_lshlrev_b32 v2, 4, v1\n"
      "s_waitcnt lgkmcnt(0)\n"
      "s_barrier\n"
      "s_branch LAB%=\n"
      "LA0%=:\n"
      "v_lshrrev_b32 v9, 2, v1\n"
      "v_and_b32 v9, 4, v9\n"
      "v_xor_b32 v9, v9, v1\n"
      "v_and_b32 v9, 7, v9\n"
      "v_lshlrev_b32 v9, 4, v9\n"
      "v_lshrrev_b32 v10, 3, v1\n"
      "v_lshl_or_b32 v9, v10, 10, v9\n"
      "v_add_u32 v10, 7168, v9\n"
      "s_lshl_b32 s42, s62, 7\n"
      "s_lshl2_add_u32 s78, s79, s42\n"
      "s_lshl1_add_u32 s79, s53, 32768\n"
      "s_lshl2_add_u32 s59, s61, 256\n"
      "s_add_u32 s53, s53, 98304\n"
      "s_add_u32 s42, s65, 1\n"
      "s_and_b32 s42, s42, 3\n"
      "s_lshl_b32 s42, s42, 12\n"
      "s_add_u32 s80, s42, s53\n"
      "s_lshl2_add_u32 s83, s42, s52\n"
      "s_add_u32 s42, s65, 2\n"
      "s_and_b32 s42, s42, 3\n"
      "s_lshl_b32 s42, s42, 12\n"
      "s_add_u32 s81, s42, s53\n"
      "s_lshl2_add_u32 s84, s42, s52\n"
      "s_add_u32 s42, s65, 3\n"
      "s_and_b32 s42, s42, 3\n"
      "s_lshl_b32 s42, s42, 12\n"
      "s_add_u32 s82, s42, s53\n"
      "s_lshl2_add_u32 s85, s42, s52\n"
      "s_mov_b32 s86, s64\n"
      "s_mov_b32 s87, 0\n"
      "s_add_u32 s42, s62, s61\n"
      "v_lshrrev_b32 v11, 5, v1\n"
      "v_lshl_add_u32 v11, v11, 2, s42\n"
      "v_and_b32 v11, 7, v11\n"
      "v_and_b32 v12, 31, v1\n"
      "v_lshlrev_b32 v12, 3, v12\n"
      "v_lshl_or_b32 v11, v11, 8, v12\n"
      "s_and_b32 s41, %[bid], 1\n"
      "s_lshl_b32 s41, s41, 11\n"
      "s_add_u32 s41, s41, 4096\n"
      "s_waitcnt lgkmcnt(0)\n"
      "s_barrier\n"
      "buffer_load_dwordx2 v[106:107], off, s[16:19], 0 sc1\n"
      "LA1%=:\n"
      "s_lshl_b32 s53, s86, 8\n"
      "s_add_u32 m0, s80, s87\n"
      "s_add_u32 s42, s83, s53\n"
      "buffer_load_dwordx4 v8, s[8:11], s42 offen lds\n"
      "s_add_u32 m0, s81, s87\n"
      "s_add_u32 s42, s84, s53\n"
      "buffer_load_dwordx4 v8, s[8:11], s42 offen lds\n"
      "s_add_u32 m0, s82, s87\n"
      "s_add_u32 s42, s85, s53\n"
      "buffer_load_dwordx4 v8, s[8:11], s42 offen lds\n"
      "s_lshl_b32 s42, s86, 17\n"
      "s_add_u32 m0, s79, s87\n"
      "s_add_u32 s42, s42, s78\n"
      "buffer_load_dwordx4 v9, s[12:15], s42 offen offset:0 lds\n"
      "buffer_load_dwordx4 v10, s[12:15], s42 offen offset:1024 lds\n"
      "s_lshr_b32 s43, s87, 13\n"
      "s_bitcmp1_b32 234, s43\n"
      "s_cbranch_scc0 LA5%=\n"
      "s_mov_b32 m0, s59\n"
      "s_mov_b64 exec, 1\n"
      "global_load_lds_dword v158, s[88:89]\n"
      "s_mov_b64 exec, -1\n"
      "LA5%=:\n"
      "s_add_u32 s53, s79, s87\n"
      "s_add_u32 m0, s53, 0x2000\n"
      "s_add_u32 s42, s42, 0x10000\n"
      "buffer_load_dwordx4 v9, s[12:15], s42 offen offset:0 lds\n"
      "buffer_load_dwordx4 v10, s[12:15], s42 offen offset:1024 lds\n"
      "s_add_u32 m0, s59, 16\n"
      "s_mov_b64 exec, 1\n"
      "global_load_lds_dword v158, s[88:89]\n"
      "s_mov_b64 exec, -1\n"
      "s_add_u32 s86, s86, 1\n"
      "s_and_b32 s86, s86, 3\n"
      "s_add_u32 s59, s59, 32\n"
      "s_add_u32 s87, s87, 0x4000\n"
      "s_cmp_lg_u32 s87, 0x10000\n"
      "s_cbranch_scc1 LA1%=\n"
      "v_lshlrev_b32 v163, 2, v1\n"
      "s_branch LAB%=\n"
      "LS2%=:\n"
      "v_mov_b32 v157, 1\n"
      "v_lshlrev_b32 v163, 2, v1\n"
      "s_lshl_b32 s42, s62, 7\n"
      "v_lshl_add_u32 v18, v1, 1, s42\n"
      "s_waitcnt lgkmcnt(0)\n"
      "global_load_ushort v152, v18, s[28:29]\n"
      "global_load_ushort v153, v18, s[30:31]\n"
      "global_load_ushort v154, v18, s[32:33]\n"
      "v_and_b32 v18, 31, v1\n"
      "s_lshr_b32 s43, s61, 1\n"
      "s_lshl_b32 s43, s43, 6\n"
      "s_add_u32 s42, s42, s43\n"
      "v_lshl_add_u32 v18, v18, 1, s42\n"
      "global_load_ushort v155, v18, s[34:35]\n"
      "s_cmp_lg_u32 s61, 0\n"
      "s_cbranch_scc1 LSX%=\n"
      "v_and_b32 v18, 15, v1\n"
      "v_and_b32 v8, 0xf0, v8\n"
      "v_lshl_or_b32 v8, v18, 8, v8\n"
      "v_add_u32 v8, s45, v8\n"
      "v_xor_b32 v9, 64, v8\n"
      "v_xor_b32 v10, 128, v8\n"
      "v_xor_b32 v11, 192, v8\n"
      "v_lshrrev_b32 v19, 4, v1\n"
      "v_lshlrev_b32 v12, 3, v18\n"
      "v_lshl_or_b32 v12, v19, 8, v12\n"
      "v_add_u32 v12, s46, v12\n"
      "s_bitcmp1_b32 s62, 0\n"
      "s_cselect_b32 s42, 7, 9\n"
      "s_cselect_b32 s43, 16, 32\n"
      "s_cselect_b32 s54, 1.0, 0x3eb8aa3b\n"
      "s_mov_b32 s55, s54\n"
      "v_lshlrev_b32 v13, s42, v19\n"
      "v_lshl_or_b32 v13, v18, 4, v13\n"
      "v_and_b32 v19, s43, v1\n"
      "v_lshl_or_b32 v13, v19, 24, v13\n"
      "v_lshlrev_b32 v18, 11, v1\n"
      "v_and_b32 v19, 12, v163\n"
      "s_lshl_b32 s42, s63, 6\n"
      "s_lshl3_add_u32 s42, s62, s42\n"
      "s_add_u32 s42, s42, 4096\n"
      "s_waitcnt vmcnt(13)\n"
      "v_readfirstlane_b32 s74, v16\n"
      "v_readfirstlane_b32 s75, v17\n"
      "s_xor_b32 s50, s74, 0x7f4a7c15\n"
      "s_xor_b32 s51, s75, 0x9e3779b9\n"
      "s_mul_i32 s73, s50, 0xd6e8feb8\n"
      "s_mul_hi_u32 s52, s50, 0x6659fd93\n"
      "s_mul_i32 s51, s51, 0x6659fd93\n"
      "s_mul_i32 s72, s50, 0x6659fd93\n"
      "s_add_u32 s73, s73, s52\n"
      "s_add_u32 s73, s73, s51\n"
      "v_mov_b32 v16, s72\n"
      "v_mov_b32 v17, s73\n"
      "s_mov_b32 s40, 200000\n"
      "Lfs%=:\n"
      "ds_read_b32 v159, v19 offset:384\n"
      "s_waitcnt lgkmcnt(0)\n"
      "v_cmp_eq_u32 vcc, 0, v159\n"
      "s_sub_u32 s40, s40, 1\n"
      "s_cbranch_vccz Lfsok%=\n"
      "s_cbranch_scc0 Lfs%=\n"
      "Lfsok%=:\n"
      "s_bitcmp1_b32 s62, 0\n"
      "s_cbranch_scc1 LSV%=\n"
      "ds_read_b128 v[24:27], v8 offset:0\n"
      "ds_read_b64_tr_b16 v[28:29], v12 offset:0\n"
      "ds_read_b64_tr_b16 v[30:31], v12 offset:128\n"
      "ds_read_b128 v[32:35], v9 offset:0\n"
      "ds_read_b64_tr_b16 v[36:37], v12 offset:1024\n"
      "ds_read_b64_tr_b16 v[38:39], v12 offset:1152\n"
      "ds_read_b128 v[40:43], v10 offset:0\n"
      "ds_read_b64_tr_b16 v[44:45], v12 offset:2048\n"
      "ds_read_b64_tr_b16 v[46:47], v12 offset:2176\n"
      "ds_read_b128 v[48:51], v11 offset:0\n"
      "ds_read_b64_tr_b16 v[52:53], v12 offset:3072\n"
      "ds_read_b64_tr_b16 v[54:55], v12 offset:3200\n"
      "ds_read_b128 v[56:59], v8 offset:16384\n"
      "ds_read_b64_tr_b16 v[60:61], v12 offset:4096\n"
      "ds_read_b64_tr_b16 v[62:63], v12 offset:4224\n"
      "s_waitcnt lgkmcnt(12)\n"
      "v_mfma_f32_16x16x32_bf16 v[20:23], v[28:31], v[24:27], 0\n"
      "ds_read_b128 v[24:27], v9 offset:16384\n"
      "ds_read_b64_tr_b16 v[28:29], v12 offset:5120\n"
      "ds_read_b64_tr_b16 v[30:31], v12 offset:5248\n"
      "s_waitcnt lgkmcnt(12)\n"
      "v_mfma_f32_16x16x32_bf16 v[20:23], v[36:39], v[32:35], v[20:23]\n"
      "ds_read_b128 v[32:35], v10 offset:16384\n"
      "ds_read_b64_tr_b16 v[36:37], v12 offset:6144\n"
      "ds_read_b64_tr_b16 v[38:39], v12 offset:6272\n"
      "s_waitcnt lgkmcnt(12)\n"
      "v_mfma_f32_16x16x32_bf16 v[20:23], v[44:47], v[40:43], v[20:23]\n"
      "ds_read_b128 v[40:43], v11 offset:16384\n"
      "ds_read_b64_tr_b16 v[44:45], v12 offset:7168\n"
      "ds_read_b64_tr_b16 v[46:47], v12 offset:7296\n"
      "s_waitcnt lgkmcnt(12)\n"
      "v_mfma_f32_16x16x32_bf16 v[20:23], v[52:55], v[48:51], v[20:23]\n"
      "ds_read_b128 v[48:51], v8 offset:32768\n"
      "ds_read_b64_tr_b16 v[52:53], v12 offset:8192\n"
      "ds_read_b64_tr_b16 v[54:55], v12 offset:8320\n"
      "s_waitcnt lgkmcnt(12)\n"
      "v_mfma_f32_16x16x32_bf16 v[20:23], v[60:63], v[56:59], v[20:23]\n"
      "ds_read_b128 v[56:59], v9 offset:32768\n"
      "ds_read_b64_tr_b16 v[60:61], v12 offset:9216\n"
      "ds_read_b64_tr_b16 v[62:63], v12 offset:9344\n"
      "s_waitcnt lgkmcnt(12)\n"
      "v_mfma_f32_16x16x32_bf16 v[20:23], v[28:31], v[24:27], v[20:23]\n"
      "ds_read_b128 v[24:27], v10 offset:32768\n"
      "ds_read_b64_tr_b16 v[28:29], v12 offset:10240\n"
      "ds_read_b64_tr_b16 v[30:31], v12 offset:10368\n"
      "s_mov_b64 exec, 3\n"
      "buffer_store_dwordx2 v[16:17], v18, s[16:19], s42 offen sc1\n"
      "s_mov_b64 exec, -1\n"
      "s_waitcnt lgkmcnt(12)\n"
      "v_mfma_f32_16x16x32_bf16 v[20:23], v[36:39], v[32:35], v[20:23]\n"
      "ds_read_b128 v[32:35], v11 offset:32768\n"
      "ds_read_b64_tr_b16 v[36:37], v12 offset:11264\n"
      "ds_read_b64_tr_b16 v[38:39], v12 offset:11392\n"
      "s_waitcnt lgkmcnt(12)\n"
      "v_mfma_f32_16x16x32_bf16 v[20:23], v[44:47], v[40:43], v[20:23]\n"
      "ds_read_b128 v[40:43], v8 offset:49152\n"
      "ds_read_b64_tr_b16 v[44:45], v12 offset:12288\n"
      "ds_read_b64_tr_b16 v[46:47], v12 offset:12416\n"
      "s_waitcnt lgkmcnt(12)\n"
      "v_mfma_f32_16x16x32_bf16 v[20:23], v[52:55], v[48:51], v[20:23]\n"
      "ds_read_b128 v[48:51], v9 offset:49152\n"
      "ds_read_b64_tr_b16 v[52:53], v12 offset:13312\n"
      "ds_read_b64_tr_b16 v[54:55], v12 offset:13440\n"
      "s_waitcnt lgkmcnt(12)\n"
      "v_mfma_f32_16x16x32_bf16 v[20:23], v[60:63], v[56:59], v[20:23]\n"
      "ds_read_b128 v[56:59], v10 offset:49152\n"
      "ds_read_b64_tr_b16 v[60:61], v12 offset:14336\n"
      "ds_read_b64_tr_b16 v[62:63], v12 offset:14464\n"
      "s_waitcnt lgkmcnt(12)\n"
      "v_mfma_f32_16x16x32_bf16 v[20:23], v[28:31], v[24:27], v[20:23]\n"
      "ds_read_b128 v[24:27], v11 offset:49152\n"
      "ds_read_b64_tr_b16 v[28:29], v12 offset:15360\n"
      "ds_read_b64_tr_b16 v[30:31], v12 offset:15488\n"
      "s_waitcnt lgkmcnt(12)\n"
      "v_mfma_f32_16x16x32_bf16 v[20:23], v[36:39], v[32:35], v[20:23]\n"
      "s_waitcnt lgkmcnt(9)\n"
      "v_mfma_f32_16x16x32_bf16 v[20:23], v[44:47], v[40:43], v[20:23]\n"
      "s_waitcnt lgkmcnt(6)\n"
      "v_mfma_f32_16x16x32_bf16 v[20:23], v[52:55], v[48:51], v[20:23]\n"
      "s_waitcnt lgkmcnt(3)\n"
      "v_mfma_f32_16x16x32_bf16 v[20:23], v[60:63], v[56:59], v[20:23]\n"
      "s_waitcnt lgkmcnt(0)\n"
      "v_mfma_f32_16x16x32_bf16 v[20:23], v[28:31], v[24:27], v[20:23]\n"
      "s_nop 7\n"
      "v_pk_mul_f32 v[20:21], v[20:21], s[54:55]\n"
      "v_pk_mul_f32 v[22:23], v[22:23], s[54:55]\n"
      "v_cvt_pk_bf16_f32 v20, v20, v21\n"
      "v_cvt_pk_bf16_f32 v21, v22, v23\n"
      "v_min_u32 v22, -2, v20\n"
      "v_min_u32 v23, -2, v21\n"
      "v_min_u32 v20, -2, v20\n"
      "v_min_u32 v21, -2, v21\n"
      "s_nop 0\n"
      "v_permlane32_swap_b32 v20, v22\n"
      "v_permlane32_swap_b32 v21, v23\n"
      "s_branch LS7%=\n"
      "LSV%=:\n"
      "ds_read_b128 v[24:27], v8 offset:0\n"
      "ds_read_b64_tr_b16 v[28:29], v12 offset:0\n"
      "ds_read_b64_tr_b16 v[30:31], v12 offset:128\n"
      "ds_read_b128 v[32:35], v9 offset:0\n"
      "ds_read_b64_tr_b16 v[36:37], v12 offset:1024\n"
      "ds_read_b64_tr_b16 v[38:39], v12 offset:1152\n"
      "ds_read_b128 v[40:43], v10 offset:0\n"
      "ds_read_b64_tr_b16 v[44:45], v12 offset:2048\n"
      "ds_read_b64_tr_b16 v[46:47], v12 offset:2176\n"
      "ds_read_b128 v[48:51], v11 offset:0\n"
      "ds_read_b64_tr_b16 v[52:53], v12 offset:3072\n"
      "ds_read_b64_tr_b16 v[54:55], v12 offset:3200\n"
      "ds_read_b128 v[56:59], v8 offset:16384\n"
      "ds_read_b64_tr_b16 v[60:61], v12 offset:4096\n"
      "ds_read_b64_tr_b16 v[62:63], v12 offset:4224\n"
      "s_waitcnt lgkmcnt(12)\n"
      "v_mfma_f32_16x16x32_bf16 v[20:23], v[24:27], v[28:31], 0\n"
      "ds_read_b128 v[24:27], v9 offset:16384\n"
      "ds_read_b64_tr_b16 v[28:29], v12 offset:5120\n"
      "ds_read_b64_tr_b16 v[30:31], v12 offset:5248\n"
      "s_waitcnt lgkmcnt(12)\n"
      "v_mfma_f32_16x16x32_bf16 v[20:23], v[32:35], v[36:39], v[20:23]\n"
      "ds_read_b128 v[32:35], v10 offset:16384\n"
      "ds_read_b64_tr_b16 v[36:37], v12 offset:6144\n"
      "ds_read_b64_tr_b16 v[38:39], v12 offset:6272\n"
      "s_waitcnt lgkmcnt(12)\n"
      "v_mfma_f32_16x16x32_bf16 v[20:23], v[40:43], v[44:47], v[20:23]\n"
      "ds_read_b128 v[40:43], v11 offset:16384\n"
      "ds_read_b64_tr_b16 v[44:45], v12 offset:7168\n"
      "ds_read_b64_tr_b16 v[46:47], v12 offset:7296\n"
      "s_waitcnt lgkmcnt(12)\n"
      "v_mfma_f32_16x16x32_bf16 v[20:23], v[48:51], v[52:55], v[20:23]\n"
      "ds_read_b128 v[48:51], v8 offset:32768\n"
      "ds_read_b64_tr_b16 v[52:53], v12 offset:8192\n"
      "ds_read_b64_tr_b16 v[54:55], v12 offset:8320\n"
      "s_waitcnt lgkmcnt(12)\n"
      "v_mfma_f32_16x16x32_bf16 v[20:23], v[56:59], v[60:63], v[20:23]\n"
      "ds_read_b128 v[56:59], v9 offset:32768\n"
      "ds_read_b64_tr_b16 v[60:61], v12 offset:9216\n"
      "ds_read_b64_tr_b16 v[62:63], v12 offset:9344\n"
      "s_waitcnt lgkmcnt(12)\n"
      "v_mfma_f32_16x16x32_bf16 v[20:23], v[24:27], v[28:31], v[20:23]\n"
      "ds_read_b128 v[24:27], v10 offset:32768\n"
      "ds_read_b64_tr_b16 v[28:29], v12 offset:10240\n"
      "ds_read_b64_tr_b16 v[30:31], v12 offset:10368\n"
      "s_mov_b64 exec, 3\n"
      "buffer_store_dwordx2 v[16:17], v18, s[16:19], s42 offen sc1\n"
      "s_mov_b64 exec, -1\n"
      "s_waitcnt lgkmcnt(12)\n"
      "v_mfma_f32_16x16x32_bf16 v[20:23], v[32:35], v[36:39], v[20:23]\n"
      "ds_read_b128 v[32:35], v11 offset:32768\n"
      "ds_read_b64_tr_b16 v[36:37], v12 offset:11264\n"
      "ds_read_b64_tr_b16 v[38:39], v12 offset:11392\n"
      "s_waitcnt lgkmcnt(12)\n"
      "v_mfma_f32_16x16x32_bf16 v[20:23], v[40:43], v[44:47], v[20:23]\n"
      "ds_read_b128 v[40:43], v8 offset:49152\n"
      "ds_read_b64_tr_b16 v[44:45], v12 offset:12288\n"
      "ds_read_b64_tr_b16 v[46:47], v12 offset:12416\n"
      "s_waitcnt lgkmcnt(12)\n"
      "v_mfma_f32_16x16x32_bf16 v[20:23], v[48:51], v[52:55], v[20:23]\n"
      "ds_read_b128 v[48:51], v9 offset:49152\n"
      "ds_read_b64_tr_b16 v[52:53], v12 offset:13312\n"
      "ds_read_b64_tr_b16 v[54:55], v12 offset:13440\n"
      "s_waitcnt lgkmcnt(12)\n"
      "v_mfma_f32_16x16x32_bf16 v[20:23], v[56:59], v[60:63], v[20:23]\n"
      "ds_read_b128 v[56:59], v10 offset:49152\n"
      "ds_read_b64_tr_b16 v[60:61], v12 offset:14336\n"
      "ds_read_b64_tr_b16 v[62:63], v12 offset:14464\n"
      "s_waitcnt lgkmcnt(12)\n"
      "v_mfma_f32_16x16x32_bf16 v[20:23], v[24:27], v[28:31], v[20:23]\n"
      "ds_read_b128 v[24:27], v11 offset:49152\n"
      "ds_read_b64_tr_b16 v[28:29], v12 offset:15360\n"
      "ds_read_b64_tr_b16 v[30:31], v12 offset:15488\n"
      "s_waitcnt lgkmcnt(12)\n"
      "v_mfma_f32_16x16x32_bf16 v[20:23], v[32:35], v[36:39], v[20:23]\n"
      "s_waitcnt lgkmcnt(9)\n"
      "v_mfma_f32_16x16x32_bf16 v[20:23], v[40:43], v[44:47], v[20:23]\n"
      "s_waitcnt lgkmcnt(6)\n"
      "v_mfma_f32_16x16x32_bf16 v[20:23], v[48:51], v[52:55], v[20:23]\n"
      "s_waitcnt lgkmcnt(3)\n"
      "v_mfma_f32_16x16x32_bf16 v[20:23], v[56:59], v[60:63], v[20:23]\n"
      "s_waitcnt lgkmcnt(0)\n"
      "v_mfma_f32_16x16x32_bf16 v[20:23], v[24:27], v[28:31], v[20:23]\n"
      "s_nop 7\n"
      "v_pk_mul_f32 v[20:21], v[20:21], s[54:55]\n"
      "v_pk_mul_f32 v[22:23], v[22:23], s[54:55]\n"
      "v_cvt_pk_bf16_f32 v20, v20, v21\n"
      "v_cvt_pk_bf16_f32 v21, v22, v23\n"
      "v_min_u32 v22, -2, v20\n"
      "v_min_u32 v23, -2, v21\n"
      "v_min_u32 v20, -2, v20\n"
      "v_min_u32 v21, -2, v21\n"
      "s_nop 0\n"
      "v_permlane16_swap_b32 v20, v22\n"
      "v_permlane16_swap_b32 v21, v23\n"
      "LS7%=:\n"
      "buffer_store_dwordx4 v[20:23], v13, s[16:19], s71 offen sc1\n"
      "s_add_u32 s74, s74, 1\n"
      "s_addc_u32 s75, s75, 0\n"
      "v_mov_b32 v26, s74\n"
      "v_mov_b32 v27, s75\n"
      "v_mov_b64 v[24:25], v[16:17]\n"
      "s_mov_b32 s40, 200000\n"
      "Lf7%=:\n"
      "ds_read_b32 v159, v19 offset:368\n"
      "s_waitcnt lgkmcnt(0)\n"
      "v_cmp_eq_u32 vcc, 0, v159\n"
      "s_sub_u32 s40, s40, 1\n"
      "s_cbranch_vccz Lf7ok%=\n"
      "s_sleep 1\n"
      "s_cbranch_scc0 Lf7%=\n"
      "Lf7ok%=:\n"
      "ds_write_b128 v158, v[24:27] offset:48\n"
      "s_mov_b64 exec, 1\n"
      "ds_add_u32 v158, v157 offset:96\n"
      "s_mov_b64 exec, -1\n"
      "LSX%=:\n"
      "s_mov_b32 %[bad], 0\n"
      "s_waitcnt vmcnt(0)\n"
      "s_branch Lout%=\n"
      "LAB%=:\n"
      "v_lshrrev_b32 v3, 5, v1\n"
      "v_and_b32 v161, 0xf0, v2\n"
      "v_lshl_or_b32 v3, v3, 8, v161\n"
      "v_and_b32 v161, 16, v1\n"
      "v_lshl_or_b32 v3, v161, 5, v3\n"
      "s_bfe_u32 s66, s60, 0x10002\n"
      "v_and_b32 v4, 11, v1\n"
      "s_mov_b32 s51, 0\n"
      "v_cmp_eq_u32 vcc, 0, v4\n"
      "s_mov_b32 %[bad], 0\n"
      "v_mov_b32 v4, 0x3f803f80\n"
      "s_lshl_b32 s70, s61, 1\n"
      "s_nop 0\n"
      "s_movk_i32 s42, 256\n"
      "v_cndmask_b32 v40, 0, v4, vcc\n"
      "s_movk_i32 s43, 0\n"
      "v_cndmask_b32 v41, 0, v4, vcc\n"
      "s_cmp_eq_u32 s66, 0\n"
      "v_mov_b64 v[42:43], v[40:41]\n"
      "s_cselect_b32 s58, 2, 1\n"
      "v_mov_b64 v[44:45], v[40:41]\n"
      "s_cselect_b32 s67, 18432, 0\n"
      "v_mov_b64 v[46:47], v[40:41]\n"
      "s_cselect_b32 s68, 32, s42\n"
      "v_mov_b64 v[48:49], v[40:41]\n"
      "s_cselect_b32 s69, 128, s43\n"
      "v_mov_b64 v[50:51], v[40:41]\n"
      "s_cselect_b32 s55, 16, 0\n"
      "v_mov_b64 v[52:53], v[40:41]\n"
      "s_bitset1_b32 s68, 7\n"
      "v_mov_b64 v[54:55], v[40:41]\n"
      "s_lshl_b32 s57, s67, 1\n"
      "v_mov_b64 v[56:57], v[40:41]\n"
      "s_sub_u32 s57, 18432, s57\n"
      "v_mov_b64 v[58:59], v[40:41]\n"
      "s_add_u32 s67, s67, 9216\n"
      "v_mov_b64 v[60:61], v[40:41]\n"
      "s_add_u32 s55, s55, 64\n"
      "v_mov_b64 v[62:63], v[40:41]\n"
      "s_lshl2_add_u32 s54, s61, s55\n"
      "v_mov_b64 v[64:65], v[40:41]\n"
      "s_add_u32 s43, s61, 1\n"
      "v_mov_b64 v[66:67], v[40:41]\n"
      "s_and_b32 s43, s43, 3\n"
      "v_mov_b64 v[68:69], v[40:41]\n"
      "s_lshl2_add_u32 s55, s43, s55\n"
      "v_mov_b64 v[70:71], v[40:41]\n"
      "s_add_u32 s46, s60, s62\n"
      "v_mov_b64 v[72:73], v[40:41]\n"
      "s_and_b32 s46, s46, 7\n"
      "v_mov_b64 v[74:75], v[40:41]\n"
      "s_lshl_b32 s46, s46, 14\n"
      "v_mov_b64 v[76:77], v[40:41]\n"
      "s_add_u32 s46, s46, 262144\n"
      "v_mov_b64 v[78:79], v[40:41]\n"
      "s_add_u32 s47, s46, 4096\n"
      "v_mov_b64 v[80:81], v[40:41]\n"
      "s_add_u32 s48, s46, 8192\n"
      "v_mov_b64 v[82:83], v[40:41]\n"
      "s_add_u32 s49, s46, 12288\n"
      "v_mov_b64 v[84:85], v[40:41]\n"
      "s_add_u32 s42, s61, 3\n"
      "v_mov_b64 v[86:87], v[40:41]\n"
      "s_bfe_u32 s43, s42, 0x10001\n"
      "v_mov_b64 v[88:89], v[40:41]\n"
      "s_lshl1_add_u32 s43, s63, s43\n"
      "v_mov_b64 v[90:91], v[40:41]\n"
      "s_lshl3_add_u32 s43, s43, s62\n"
      "v_mov_b64 v[92:93], v[40:41]\n"
      "s_and_b32 s42, s42, 1\n"
      "v_mov_b64 v[94:95], v[40:41]\n"
      "s_lshl2_add_u32 s43, s43, s42\n"
      "v_mov_b64 v[96:97], v[40:41]\n"
      "s_lshl_b32 s43, s43, 10\n"
      "v_mov_b64 v[98:99], v[40:41]\n"
      "s_add_u32 s56, s43, 1048576\n"
      "v_mov_b64 v[100:101], v[40:41]\n"
      "s_add_u32 s42, s61, 3\n"
      "v_mov_b64 v[102:103], v[40:41]\n"
      "s_and_b32 s42, s42, 2\n"
      "s_lshr_b32 s43, s62, 2\n"
      "s_or_b32 s42, s42, s43\n"
      "s_lshl3_add_u32 s42, s63, s42\n"
      "s_lshl_b32 s42, s42, 7\n"
      "s_add_u32 s37, s42, 32768\n"
      "s_and_b32 s42, s62, 3\n"
      "s_lshl_b32 s42, s42, 3\n"
      "s_lshl_b32 s38, 1, s42\n"
      "v_mov_b32 v6, s54\n"
      "v_mov_b32 v7, s55\n"
      "v_mov_b32 v156, 0xdddd\n"
      "v_mov_b32 v157, 1\n"
      "s_lshr_b32 s50, s61, 1\n"
      "s_mul_i32 s50, s50, 0x8040\n"
      "s_add_u32 s50, s50, 98304\n"
      "v_bfe_u32 v21, v1, 2, 2\n"
      "v_lshrrev_b32 v20, 5, v1\n"
      "v_lshlrev_b32 v20, 9, v20\n"
      "v_lshl_or_b32 v20, v21, 7, v20\n"
      "v_bfe_u32 v21, v1, 3, 1\n"
      "v_lshl_or_b32 v20, v21, 6, v20\n"
      "v_bfe_u32 v21, v1, 4, 1\n"
      "v_lshl_or_b32 v20, v21, 5, v20\n"
      "v_and_b32 v21, 3, v1\n"
      "v_lshl_or_b32 v20, v21, 3, v20\n"
      "v_xor_b32 v0, s50, v20\n"
      "s_cmp_lg_u32 s66, 0\n"
      "s_cbranch_scc1 LB1%=\n"
      "buffer_load_dwordx2 v[104:105], v11, s[16:19], s41 offen sc1\n"
      "s_waitcnt vmcnt(28)\n"
      "v_readfirstlane_b32 s74, v106\n"
      "v_readfirstlane_b32 s75, v107\n"
      "s_xor_b32 s50, s74, 0x7f4a7c15\n"
      "s_xor_b32 s53, s75, 0x9e3779b9\n"
      "s_mul_i32 s73, s50, 0xd6e8feb8\n"
      "s_mul_hi_u32 s52, s50, 0x6659fd93\n"
      "s_mul_i32 s53, s53, 0x6659fd93\n"
      "s_mul_i32 s72, s50, 0x6659fd93\n"
      "s_add_u32 s73, s73, s52\n"
      "s_add_u32 s73, s73, s53\n"
      "s_mov_b32 s40, 200000\n"
      "s_waitcnt vmcnt(0)\n"
      "LR%=:\n"
      "v_cmp_ne_u64 vcc, s[72:73], v[104:105]\n"
      "s_sub_u32 s40, s40, 1\n"
      "s_cbranch_vccz LRok%=\n"
      "s_sleep 3\n"
      "buffer_load_dwordx2 v[104:105], v11, s[16:19], s41 offen sc1\n"
      "s_waitcnt vmcnt(0)\n"
      "s_cbranch_scc0 LR%=\n"
      "LRok%=:\n"
      "Lagain%=:\n"
      "s_cmp_lg_u32 s66, 0\n"
      "s_cbranch_scc1 LBl%=\n"
      "buffer_load_dwordx4 v[8:11], v2, s[16:19], s46 offen offset:0 sc1\n"
      "buffer_load_dwordx4 v[12:15], v2, s[16:19], s46 offen offset:2048 sc1\n"
      "buffer_load_dwordx4 v[40:43], v3, s[16:19], s46 offen offset:1024 sc1\n"
      "buffer_load_dwordx4 v[16:19], v2, s[16:19], s47 offen offset:0 sc1\n"
      "buffer_load_dwordx4 v[48:51], v3, s[16:19], s46 offen offset:3072 sc1\n"
      "buffer_load_dwordx4 v[20:23], v2, s[16:19], s47 offen offset:2048 sc1\n"
      "buffer_load_dwordx4 v[56:59], v3, s[16:19], s47 offen offset:1024 sc1\n"
      "buffer_load_dwordx4 v[24:27], v2, s[16:19], s48 offen offset:0 sc1\n"
      "buffer_load_dwordx4 v[64:67], v3, s[16:19], s47 offen offset:3072 sc1\n"
      "buffer_load_dwordx4 v[28:31], v2, s[16:19], s48 offen offset:2048 sc1\n"
      "buffer_load_dwordx4 v[72:75], v3, s[16:19], s48 offen offset:1024 sc1\n"
      "buffer_load_dwordx4 v[32:35], v2, s[16:19], s49 offen offset:0 sc1\n"
      "buffer_load_dwordx4 v[80:83], v3, s[16:19], s48 offen offset:3072 sc1\n"
      "buffer_load_dwordx4 v[36:39], v2, s[16:19], s49 offen offset:2048 sc1\n"
      "buffer_load_dwordx4 v[88:91], v3, s[16:19], s49 offen offset:1024 sc1\n"
      "buffer_load_dwordx4 v[96:99], v3, s[16:19], s49 offen offset:3072 sc1\n"
      "s_cmp_lg_u32 s51, 0\n"
      "s_cbranch_scc1 Lnp%=\n"
      "s_mov_b64 exec, 1\n"
      "ds_add_u32 v158, v157 offset:148\n"
      "s_mov_b64 exec, -1\n"
      "Lnp%=:\n"
      "s_branch Lok%=\n"
      "LB1%=:\n"
      "v_lshrrev_b32 v8, 5, v1\n"
      "v_xor_b32 v9, v8, v1\n"
      "v_and_b32 v9, 15, v9\n"
      "v_lshlrev_b32 v9, 4, v9\n"
      "v_and_b32 v10, 31, v1\n"
      "v_lshl_or_b32 v9, v10, 8, v9\n"
      "s_and_b32 s42, s61, 1\n"
      "s_lshl_b32 s43, s42, 13\n"
      "s_add_u32 s43, s43, 98304\n"
      "v_add_u32 v152, s43, v9\n"
      "v_xor_b32 v153, 32, v152\n"
      "v_xor_b32 v154, 64, v152\n"
      "v_xor_b32 v155, 96, v152\n"
      "v_xor_b32 v164, 128, v152\n"
      "v_xor_b32 v165, 160, v152\n"
      "v_xor_b32 v166, 192, v152\n"
      "v_xor_b32 v167, 224, v152\n"
      "s_lshr_b32 s43, s61, 1\n"
      "s_lshl_b32 s50, s43, 6\n"
      "s_add_u32 s50, s50, 32768\n"
      "v_bfe_u32 v21, v1, 2, 2\n"
      "v_lshrrev_b32 v20, 5, v1\n"
      "v_lshlrev_b32 v20, 10, v20\n"
      "v_lshl_or_b32 v20, v21, 7, v20\n"
      "v_bfe_u32 v21, v1, 3, 1\n"
      "v_lshl_or_b32 v20, v21, 6, v20\n"
      "v_bfe_u32 v21, v1, 4, 1\n"
      "v_lshl_or_b32 v20, v21, 5, v20\n"
      "v_and_b32 v21, 3, v1\n"
      "v_lshl_or_b32 v20, v21, 3, v20\n"
      "v_xor_b32 v160, s50, v20\n"
      "s_lshl2_add_u32 s42, s42, s43\n"
      "s_lshl_b32 s42, s42, 10\n"
      "v_add_u32 v161, s42, v2\n"
      "s_mov_b32 s52, 0x3eb8aa3b\n"
      "s_mov_b32 s53, 0x3eb8aa3b\n"
      "v_and_b32 v162, 12, v163\n"
      "s_mov_b32 s40, 200000\n"
      "Lls%=:\n"
      "ds_read_b32 v159, v162 offset:384\n"
      "s_waitcnt lgkmcnt(0)\n"
      "v_cmp_eq_u32 vcc, 0, v159\n"
      "s_sub_u32 s40, s40, 1\n"
      "s_cbranch_vccz Llsok%=\n"
      "s_sleep 2\n"
      "s_cbranch_scc0 Lls%=\n"
      "Llsok%=:\n"
      "s_mov_b32 s40, 200000\n"
      "Ll0%=:\n"
      "ds_read_b32 v159, v162 offset:272\n"
      "s_waitcnt lgkmcnt(0)\n"
      "v_cmp_eq_u32 vcc, 0, v159\n"
      "s_sub_u32 s40, s40, 1\n"
      "s_cbranch_vccz Ll0ok%=\n"
      "s_sleep 1\n"
      "s_cbranch_scc0 Ll0%=\n"
      "Ll0ok%=:\n"
      "ds_read_b64_tr_b16 v[8:9], v160 offset:0\n"
      "ds_read_b64_tr_b16 v[10:11], v160 offset:512\n"
      "ds_read_b128 v[104:107], v152 offset:0\n"
      "ds_read_b64_tr_b16 v[12:13], v160 offset:2048\n"
      "ds_read_b64_tr_b16 v[14:15], v160 offset:2560\n"
      "ds_read_b128 v[108:111], v153 offset:0\n"
      "ds_read_b64_tr_b16 v[16:17], v160 offset:4096\n"
      "ds_read_b64_tr_b16 v[18:19], v160 offset:4608\n"
      "ds_read_b128 v[112:115], v154 offset:0\n"
      "ds_read_b64_tr_b16 v[20:21], v160 offset:6144\n"
      "ds_read_b64_tr_b16 v[22:23], v160 offset:6656\n"
      "ds_read_b128 v[116:119], v155 offset:0\n"
      "s_waitcnt lgkmcnt(9)\n"
      "v_mfma_f32_32x32x16_bf16 v[136:151], v[8:11], v[104:107], 0\n"
      "ds_read_b64_tr_b16 v[24:25], v160 offset:8192\n"
      "ds_read_b64_tr_b16 v[26:27], v160 offset:8704\n"
      "ds_read_b128 v[120:123], v164 offset:0\n"
      "s_waitcnt lgkmcnt(9)\n"
      "v_mfma_f32_32x32x16_bf16 v[136:151], v[12:15], v[108:111], v[136:151]\n"
      "ds_read_b64_tr_b16 v[28:29], v160 offset:10240\n"
      "ds_read_b64_tr_b16 v[30:31], v160 offset:10752\n"
      "ds_read_b128 v[124:127], v165 offset:0\n"
      "ds_read_b32 v159, v162 offset:304\n"
      "s_waitcnt lgkmcnt(10)\n"
      "v_mfma_f32_32x32x16_bf16 v[136:151], v[16:19], v[112:115], v[136:151]\n"
      "ds_read_b64_tr_b16 v[32:33], v160 offset:12288\n"
      "ds_read_b64_tr_b16 v[34:35], v160 offset:12800\n"
      "ds_read_b128 v[128:131], v166 offset:0\n"
      "s_waitcnt lgkmcnt(10)\n"
      "v_mfma_f32_32x32x16_bf16 v[136:151], v[20:23], v[116:119], v[136:151]\n"
      "ds_read_b64_tr_b16 v[36:37], v160 offset:14336\n"
      "ds_read_b64_tr_b16 v[38:39], v160 offset:14848\n"
      "ds_read_b128 v[132:135], v167 offset:0\n"
      "s_waitcnt lgkmcnt(10)\n"
      "v_mfma_f32_32x32x16_bf16 v[136:151], v[24:27], v[120:123], v[136:151]\n"
      "s_waitcnt lgkmcnt(6)\n"
      "v_cmp_eq_u32 vcc, 0, v159\n"
      "s_cbranch_vccz Ll2ok%=\n"
      "s_mov_b32 s40, 200000\n"
      "Ll2%=:\n"
      "ds_read_b32 v159, v162 offset:304\n"
      "s_waitcnt lgkmcnt(0)\n"
      "v_cmp_eq_u32 vcc, 0, v159\n"
      "s_sub_u32 s40, s40, 1\n"
      "s_cbranch_vccz Ll2ok%=\n"
      "s_cbranch_scc0 Ll2%=\n"
      "Ll2ok%=:\n"
      "ds_read_b64_tr_b16 v[8:9], v160 offset:16384\n"
      "ds_read_b64_tr_b16 v[10:11], v160 offset:16896\n"
      "ds_read_b128 v[104:107], v152 offset:16384\n"
      "s_waitcnt lgkmcnt(10)\n"
      "v_mfma_f32_32x32x16_bf16 v[136:151], v[28:31], v[124:127], v[136:151]\n"
      "ds_read_b64_tr_b16 v[12:13], v160 offset:18432\n"
      "ds_read_b64_tr_b16 v[14:15], v160 offset:18944\n"
      "ds_read_b128 v[108:111], v153 offset:16384\n"
      "s_waitcnt lgkmcnt(9)\n"
      "v_mfma_f32_32x32x16_bf16 v[136:151], v[32:35], v[128:131], v[136:151]\n"
      "ds_read_b64_tr_b16 v[16:17], v160 offset:20480\n"
      "ds_read_b64_tr_b16 v[18:19], v160 offset:20992\n"
      "ds_read_b128 v[112:115], v154 offset:16384\n"
      "s_waitcnt lgkmcnt(9)\n"
      "v_mfma_f32_32x32x16_bf16 v[136:151], v[36:39], v[132:135], v[136:151]\n"
      "ds_read_b64_tr_b16 v[20:21], v160 offset:22528\n"
      "ds_read_b64_tr_b16 v[22:23], v160 offset:23040\n"
      "ds_read_b128 v[116:119], v155 offset:16384\n"
      "s_waitcnt lgkmcnt(9)\n"
      "v_mfma_f32_32x32x16_bf16 v[136:151], v[8:11], v[104:107], v[136:151]\n"
      "ds_read_b64_tr_b16 v[24:25], v160 offset:24576\n"
      "ds_read_b64_tr_b16 v[26:27], v160 offset:25088\n"
      "ds_read_b128 v[120:123], v164 offset:16384\n"
      "s_waitcnt lgkmcnt(9)\n"
      "v_mfma_f32_32x32x16_bf16 v[136:151], v[12:15], v[108:111], v[136:151]\n"
      "ds_read_b64_tr_b16 v[28:29], v160 offset:26624\n"
      "ds_read_b64_tr_b16 v[30:31], v160 offset:27136\n"
      "ds_read_b128 v[124:127], v165 offset:16384\n"
      "ds_read_b32 v159, v162 offset:336\n"
      "s_waitcnt lgkmcnt(10)\n"
      "v_mfma_f32_32x32x16_bf16 v[136:151], v[16:19], v[112:115], v[136:151]\n"
      "ds_read_b64_tr_b16 v[32:33], v160 offset:28672\n"
      "ds_read_b64_tr_b16 v[34:35], v160 offset:29184\n"
      "ds_read_b128 v[128:131], v166 offset:16384\n"
      "s_waitcnt lgkmcnt(10)\n"
      "v_mfma_f32_32x32x16_bf16 v[136:151], v[20:23], v[116:119], v[136:151]\n"
      "ds_read_b64_tr_b16 v[36:37], v160 offset:30720\n"
      "ds_read_b64_tr_b16 v[38:39], v160 offset:31232\n"
      "ds_read_b128 v[132:135], v167 offset:16384\n"
      "s_waitcnt lgkmcnt(10)\n"
      "v_mfma_f32_32x32x16_bf16 v[136:151], v[24:27], v[120:123], v[136:151]\n"
      "s_waitcnt lgkmcnt(6)\n"
      "v_cmp_eq_u32 vcc, 0, v159\n"
      "s_cbranch_vccz Ll4ok%=\n"
      "s_mov_b32 s40, 200000\n"
      "Ll4%=:\n"
      "ds_read_b32 v159, v162 offset:336\n"
      "s_waitcnt lgkmcnt(0)\n"
      "v_cmp_eq_u32 vcc, 0, v159\n"
      "s_sub_u32 s40, s40, 1\n"
      "s_cbranch_vccz Ll4ok%=\n"
      "s_cbranch_scc0 Ll4%=\n"
      "Ll4ok%=:\n"
      "ds_read_b64_tr_b16 v[8:9], v160 offset:32768\n"
      "ds_read_b64_tr_b16 v[10:11], v160 offset:33280\n"
      "ds_read_b128 v[104:107], v152 offset:32768\n"
      "s_waitcnt lgkmcnt(10)\n"
      "v_mfma_f32_32x32x16_bf16 v[136:151], v[28:31], v[124:127], v[136:151]\n"
      "ds_read_b64_tr_b16 v[12:13], v160 offset:34816\n"
      "ds_read_b64_tr_b16 v[14:15], v160 offset:35328\n"
      "ds_read_b128 v[108:111], v153 offset:32768\n"
      "s_waitcnt lgkmcnt(9)\n"
      "v_mfma_f32_32x32x16_bf16 v[136:151], v[32:35], v[128:131], v[136:151]\n"
      "ds_read_b64_tr_b16 v[16:17], v160 offset:36864\n"
      "ds_read_b64_tr_b16 v[18:19], v160 offset:37376\n"
      "ds_read_b128 v[112:115], v154 offset:32768\n"
      "s_waitcnt lgkmcnt(9)\n"
      "v_mfma_f32_32x32x16_bf16 v[136:151], v[36:39], v[132:135], v[136:151]\n"
      "ds_read_b64_tr_b16 v[20:21], v160 offset:38912\n"
      "ds_read_b64_tr_b16 v[22:23], v160 offset:39424\n"
      "ds_read_b128 v[116:119], v155 offset:32768\n"
      "s_waitcnt lgkmcnt(9)\n"
      "v_mfma_f32_32x32x16_bf16 v[136:151], v[8:11], v[104:107], v[136:151]\n"
      "ds_read_b64_tr_b16 v[24:25], v160 offset:40960\n"
      "ds_read_b64_tr_b16 v[26:27], v160 offset:41472\n"
      "ds_read_b128 v[120:123], v164 offset:32768\n"
      "s_waitcnt lgkmcnt(9)\n"
      "v_mfma_f32_32x32x16_bf16 v[136:151], v[12:15], v[108:111], v[136:151]\n"
      "ds_read_b64_tr_b16 v[28:29], v160 offset:43008\n"
      "ds_read_b64_tr_b16 v[30:31], v160 offset:43520\n"
      "ds_read_b128 v[124:127], v165 offset:32768\n"
      "ds_read_b32 v159, v162 offset:352\n"
      "s_waitcnt lgkmcnt(10)\n"
      "v_mfma_f32_32x32x16_bf16 v[136:151], v[16:19], v[112:115], v[136:151]\n"
      "ds_read_b64_tr_b16 v[32:33], v160 offset:45056\n"
      "ds_read_b64_tr_b16 v[34:35], v160 offset:45568\n"
      "ds_read_b128 v[128:131], v166 offset:32768\n"
      "s_waitcnt lgkmcnt(10)\n"
      "v_mfma_f32_32x32x16_bf16 v[136:151], v[20:23], v[116:119], v[136:151]\n"
      "ds_read_b64_tr_b16 v[36:37], v160 offset:47104\n"
      "ds_read_b64_tr_b16 v[38:39], v160 offset:47616\n"
      "ds_read_b128 v[132:135], v167 offset:32768\n"
      "s_waitcnt lgkmcnt(10)\n"
      "v_mfma_f32_32x32x16_bf16 v[136:151], v[24:27], v[120:123], v[136:151]\n"
      "s_waitcnt lgkmcnt(6)\n"
      "v_cmp_eq_u32 vcc, 0, v159\n"
      "s_cbranch_vccz Ll6ok%=\n"
      "s_mov_b32 s40, 200000\n"
      "Ll6%=:\n"
      "ds_read_b32 v159, v162 offset:352\n"
      "s_waitcnt lgkmcnt(0)\n"
      "v_cmp_eq_u32 vcc, 0, v159\n"
      "s_sub_u32 s40, s40, 1\n"
      "s_cbranch_vccz Ll6ok%=\n"
      "s_cbranch_scc0 Ll6%=\n"
      "Ll6ok%=:\n"
      "ds_read_b64_tr_b16 v[8:9], v160 offset:49152\n"
      "ds_read_b64_tr_b16 v[10:11], v160 offset:49664\n"
      "ds_read_b128 v[104:107], v152 offset:49152\n"
      "s_waitcnt lgkmcnt(10)\n"
      "v_mfma_f32_32x32x16_bf16 v[136:151], v[28:31], v[124:127], v[136:151]\n"
      "ds_read_b64_tr_b16 v[12:13], v160 offset:51200\n"
      "ds_read_b64_tr_b16 v[14:15], v160 offset:51712\n"
      "ds_read_b128 v[108:111], v153 offset:49152\n"
      "s_waitcnt lgkmcnt(9)\n"
      "v_mfma_f32_32x32x16_bf16 v[136:151], v[32:35], v[128:131], v[136:151]\n"
      "ds_read_b64_tr_b16 v[16:17], v160 offset:53248\n"
      "ds_read_b64_tr_b16 v[18:19], v160 offset:53760\n"
      "ds_read_b128 v[112:115], v154 offset:49152\n"
      "s_waitcnt lgkmcnt(9)\n"
      "v_mfma_f32_32x32x16_bf16 v[136:151], v[36:39], v[132:135], v[136:151]\n"
      "ds_read_b64_tr_b16 v[20:21], v160 offset:55296\n"
      "ds_read_b64_tr_b16 v[22:23], v160 offset:55808\n"
      "ds_read_b128 v[116:119], v155 offset:49152\n"
      "ds_read_b32 v159, v162 offset:368\n"
      "s_waitcnt lgkmcnt(10)\n"
      "v_mfma_f32_32x32x16_bf16 v[136:151], v[8:11], v[104:107], v[136:151]\n"
      "s_waitcnt lgkmcnt(7)\n"
      "v_mfma_f32_32x32x16_bf16 v[136:151], v[12:15], v[108:111], v[136:151]\n"
      "s_waitcnt lgkmcnt(4)\n"
      "v_mfma_f32_32x32x16_bf16 v[136:151], v[16:19], v[112:115], v[136:151]\n"
      "s_waitcnt lgkmcnt(0)\n"
      "v_cmp_eq_u32 vcc, 0, v159\n"
      "s_waitcnt lgkmcnt(1)\n"
      "v_mfma_f32_32x32x16_bf16 v[136:151], v[20:23], v[116:119], v[136:151]\n"
      "s_cbranch_vccz Ll7ok%=\n"
      "s_mov_b32 s40, 200000\n"
      "Ll7%=:\n"
      "ds_read_b32 v159, v162 offset:368\n"
      "s_waitcnt lgkmcnt(0)\n"
      "v_cmp_eq_u32 vcc, 0, v159\n"
      "s_sub_u32 s40, s40, 1\n"
      "s_cbranch_vccz Ll7ok%=\n"
      "s_cbranch_scc0 Ll7%=\n"
      "Ll7ok%=:\n"
      "ds_read_b64_tr_b16 v[24:25], v160 offset:57344\n"
      "ds_read_b64_tr_b16 v[26:27], v160 offset:57856\n"
      "ds_read_b128 v[120:123], v164 offset:49152\n"
      "ds_read_b64_tr_b16 v[28:29], v160 offset:59392\n"
      "ds_read_b64_tr_b16 v[30:31], v160 offset:59904\n"
      "ds_read_b128 v[124:127], v165 offset:49152\n"
      "ds_read_b64_tr_b16 v[32:33], v160 offset:61440\n"
      "ds_read_b64_tr_b16 v[34:35], v160 offset:61952\n"
      "ds_read_b128 v[128:131], v166 offset:49152\n"
      "ds_read_b64_tr_b16 v[36:37], v160 offset:63488\n"
      "ds_read_b64_tr_b16 v[38:39], v160 offset:64000\n"
      "ds_read_b128 v[132:135], v167 offset:49152\n"
      "s_waitcnt lgkmcnt(9)\n"
      "v_mfma_f32_32x32x16_bf16 v[136:151], v[24:27], v[120:123], v[136:151]\n"
      "s_waitcnt lgkmcnt(6)\n"
      "v_mfma_f32_32x32x16_bf16 v[136:151], v[28:31], v[124:127], v[136:151]\n"
      "s_waitcnt lgkmcnt(3)\n"
      "v_mfma_f32_32x32x16_bf16 v[136:151], v[32:35], v[128:131], v[136:151]\n"
      "s_waitcnt lgkmcnt(0)\n"
      "v_mfma_f32_32x32x16_bf16 v[136:151], v[36:39], v[132:135], v[136:151]\n"
      "s_nop 7\n"
      "s_nop 3\n"
      "v_cvt_pk_bf16_f32 v104, v136, v137\n"
      "v_cvt_pk_bf16_f32 v105, v138, v139\n"
      "v_cvt_pk_bf16_f32 v106, v140, v141\n"
      "v_cvt_pk_bf16_f32 v107, v142, v143\n"
      "ds_write_b128 v161, v[104:107] offset:1024\n"
      "v_cvt_pk_bf16_f32 v108, v144, v145\n"
      "v_cvt_pk_bf16_f32 v109, v146, v147\n"
      "v_cvt_pk_bf16_f32 v110, v148, v149\n"
      "v_cvt_pk_bf16_f32 v111, v150, v151\n"
      "ds_write_b128 v161, v[108:111] offset:3072\n"
      "s_mov_b64 exec, 1\n"
      "ds_add_u32 v158, v157 offset:120\n"
      "s_mov_b64 exec, -1\n"
      "s_mov_b32 s40, 200000\n"
      "Lpb%=:\n"
      "ds_read_b32 v159, v158 offset:148\n"
      "s_waitcnt lgkmcnt(0)\n"
      "v_cmp_gt_u32 vcc, 4, v159\n"
      "s_sub_u32 s40, s40, 1\n"
      "s_cbranch_vccz Lpbok%=\n"
      "s_sleep 1\n"
      "s_cbranch_scc0 Lpb%=\n"
      "Lpbok%=:\n"
      "LBl%=:\n"
      "s_mov_b32 s44, 0xffff\n"
      "s_mov_b32 s45, 0xffff\n"
      "buffer_load_dwordx4 v[8:11], v2, s[16:19], s46 offen offset:0 sc1\n"
      "buffer_load_dwordx4 v[12:15], v2, s[16:19], s46 offen offset:2048 sc1\n"
      "s_mov_b64 exec, s[44:45]\n"
      "buffer_load_dwordx4 v[40:43], v3, s[16:19], s46 offen offset:1024 sc1\n"
      "buffer_load_dwordx4 v[44:47], v3, s[16:19], s46 offen offset:1536 sc1\n"
      "s_mov_b64 exec, -1\n"
      "buffer_load_dwordx4 v[16:19], v2, s[16:19], s47 offen offset:0 sc1\n"
      "s_mov_b64 exec, s[44:45]\n"
      "buffer_load_dwordx4 v[48:51], v3, s[16:19], s46 offen offset:3072 sc1\n"
      "buffer_load_dwordx4 v[52:55], v3, s[16:19], s46 offen offset:3584 sc1\n"
      "s_mov_b64 exec, -1\n"
      "buffer_load_dwordx4 v[20:23], v2, s[16:19], s47 offen offset:2048 sc1\n"
      "s_mov_b64 exec, s[44:45]\n"
      "buffer_load_dwordx4 v[56:59], v3, s[16:19], s47 offen offset:1024 sc1\n"
      "buffer_load_dwordx4 v[60:63], v3, s[16:19], s47 offen offset:1536 sc1\n"
      "s_mov_b64 exec, -1\n"
      "buffer_load_dwordx4 v[24:27], v2, s[16:19], s48 offen offset:0 sc1\n"
      "s_mov_b64 exec, s[44:45]\n"
      "buffer_load_dwordx4 v[64:67], v3, s[16:19], s47 offen offset:3072 sc1\n"
      "buffer_load_dwordx4 v[68:71], v3, s[16:19], s47 offen offset:3584 sc1\n"
      "s_mov_b64 exec, -1\n"
      "buffer_load_dwordx4 v[28:31], v2, s[16:19], s48 offen offset:2048 sc1\n"
      "s_mov_b64 exec, s[44:45]\n"
      "buffer_load_dwordx4 v[72:75], v3, s[16:19], s48 offen offset:1024 sc1\n"
      "buffer_load_dwordx4 v[76:79], v3, s[16:19], s48 offen offset:1536 sc1\n"
      "s_mov_b64 exec, -1\n"
      "buffer_load_dwordx4 v[32:35], v2, s[16:19], s49 offen offset:0 sc1\n"
      "s_mov_b64 exec, s[44:45]\n"
      "buffer_load_dwordx4 v[80:83], v3, s[16:19], s48 offen offset:3072 sc1\n"
      "buffer_load_dwordx4 v[84:87], v3, s[16:19], s48 offen offset:3584 sc1\n"
      "s_mov_b64 exec, -1\n"
      "buffer_load_dwordx4 v[36:39], v2, s[16:19], s49 offen offset:2048 sc1\n"
      "s_mov_b64 exec, s[44:45]\n"
      "buffer_load_dwordx4 v[88:91], v3, s[16:19], s49 offen offset:1024 sc1\n"
      "buffer_load_dwordx4 v[92:95], v3, s[16:19], s49 offen offset:1536 sc1\n"
      "s_mov_b64 exec, -1\n"
      "s_mov_b64 exec, s[44:45]\n"
      "buffer_load_dwordx4 v[96:99], v3, s[16:19], s49 offen offset:3072 sc1\n"
      "buffer_load_dwordx4 v[100:103], v3, s[16:19], s49 offen offset:3584 sc1\n"
      "s_mov_b64 exec, -1\n"
      "Lok%=:\n"
      "ds_read_b128 v[136:139], v158 offset:32\n"
      "ds_read_b128 v[140:143], v158 offset:32\n"
      "ds_read_b128 v[144:147], v158 offset:32\n"
      "s_lshl_b32 s42, s70, 10\n"
      "v_add_u32 v160, s42, v2\n"
      "s_mul_i32 s42, s70, 2304\n"
      "s_add_u32 s42, s42, s67\n"
      "v_add_u32 v161, s42, v2\n"
      "v_add_u32 v162, s42, v163\n"
      "s_mov_b32 s40, 200000\n"
      "Lq%=:\n"
      "ds_read_b32 v159, v158 offset:120\n"
      "ds_read_b128 v[152:155], v160 offset:1024\n"
      "s_waitcnt lgkmcnt(0)\n"
      "v_cmp_gt_u32 vcc, 4, v159\n"
      "s_sub_u32 s40, s40, 1\n"
      "s_cbranch_vccz Lqok%=\n"
      "s_cbranch_scc0 Lq%=\n"
      "Lqok%=:\n"
      "s_cmp_lg_u32 s51, 0\n"
      "s_cbranch_scc1 Lp1%=\n"
      "s_mov_b64 exec, 1\n"
      "ds_add_u32 v158, v157 offset:116\n"
      "s_mov_b64 exec, -1\n"
      "Lp1%=:\n"
      "s_cmp_eq_u32 s66, 0\n"
      "s_setprio 2\n"
      "s_cbranch_scc1 Lp2%=\n"
      "s_setprio 1\n"
      "Lp2%=:\n"
      "s_add_u32 s41, s70, 1\n"
      "s_mov_b32 s43, 1\n"
      "s_cmp_lg_u32 s66, 0\n"
      "s_waitcnt vmcnt(15)\n"
      "v_mfma_f32_32x32x16_bf16 v[104:119], v[8:11], v[152:155], -4.0\n"
      "s_cbranch_scc1 Lskb%=\n"
      "s_waitcnt vmcnt(13)\n"
      "v_permlane16_swap_b32 v40, v44\n"
      "v_permlane16_swap_b32 v41, v45\n"
      "v_permlane16_swap_b32 v42, v46\n"
      "v_permlane16_swap_b32 v43, v47\n"
      "s_waitcnt vmcnt(11)\n"
      "v_permlane16_swap_b32 v48, v52\n"
      "v_permlane16_swap_b32 v49, v53\n"
      "v_permlane16_swap_b32 v50, v54\n"
      "v_permlane16_swap_b32 v51, v55\n"
      "s_waitcnt vmcnt(9)\n"
      "v_permlane16_swap_b32 v56, v60\n"
      "v_permlane16_swap_b32 v57, v61\n"
      "v_permlane16_swap_b32 v58, v62\n"
      "v_permlane16_swap_b32 v59, v63\n"
      "s_waitcnt vmcnt(7)\n"
      "v_permlane16_swap_b32 v64, v68\n"
      "v_permlane16_swap_b32 v65, v69\n"
      "v_permlane16_swap_b32 v66, v70\n"
      "v_permlane16_swap_b32 v67, v71\n"
      "s_waitcnt vmcnt(5)\n"
      "v_permlane16_swap_b32 v72, v76\n"
      "v_permlane16_swap_b32 v73, v77\n"
      "v_permlane16_swap_b32 v74, v78\n"
      "v_permlane16_swap_b32 v75, v79\n"
      "s_waitcnt vmcnt(3)\n"
      "v_permlane16_swap_b32 v80, v84\n"
      "v_permlane16_swap_b32 v81, v85\n"
      "v_permlane16_swap_b32 v82, v86\n"
      "v_permlane16_swap_b32 v83, v87\n"
      "s_waitcnt vmcnt(1)\n"
      "v_permlane16_swap_b32 v88, v92\n"
      "v_permlane16_swap_b32 v89, v93\n"
      "v_permlane16_swap_b32 v90, v94\n"
      "v_permlane16_swap_b32 v91, v95\n"
      "s_waitcnt vmcnt(0)\n"
      "v_permlane16_swap_b32 v96, v100\n"
      "v_permlane16_swap_b32 v97, v101\n"
      "v_permlane16_swap_b32 v98, v102\n"
      "v_permlane16_swap_b32 v99, v103\n"
      "s_branch Lskc%=\n"
      "Lskb%=:\n"
      "s_nop 7\n"
      "s_nop 2\n"
      "Lskc%=:\n"
      "v_exp_f32 v104, v104\n"
      "v_exp_f32 v105, v105\n"
      "s_waitcnt vmcnt(14)\n"
      "v_exp_f32 v106, v106\n"
      "v_exp_f32 v107, v107\n"
      "v_exp_f32 v108, v108\n"
      "v_mfma_f32_32x32x16_bf16 v[120:135], v[12:15], v[152:155], -4.0\n"
      "s_waitcnt vmcnt(13)\n"
      "v_exp_f32 v109, v109\n"
      "v_exp_f32 v110, v110\n"
      "v_exp_f32 v111, v111\n"
      "v_exp_f32 v112, v112\n"
      "v_exp_f32 v113, v113\n"
      "v_smfmac_f32_32x32x32_bf16 v[136:151], v[40:43], v[104:111], v156\n"
      "v_exp_f32 v114, v114\n"
      "v_exp_f32 v115, v115\n"
      "v_exp_f32 v116, v116\n"
      "v_exp_f32 v117, v117\n"
      "v_exp_f32 v118, v118\n"
      "v_exp_f32 v119, v119\n"
      "v_exp_f32 v120, v120\n"
      "v_exp_f32 v121, v121\n"
      "v_smfmac_f32_32x32x32_bf16 v[136:151], v[44:47], v[112:119], v156\n"
      "s_waitcnt vmcnt(12)\n"
      "v_exp_f32 v122, v122\n"
      "v_exp_f32 v123, v123\n"
      "v_exp_f32 v124, v124\n"
      "v_mfma_f32_32x32x16_bf16 v[104:119], v[16:19], v[152:155], -4.0\n"
      "s_waitcnt vmcnt(11)\n"
      "v_exp_f32 v125, v125\n"
      "v_exp_f32 v126, v126\n"
      "v_exp_f32 v127, v127\n"
      "v_exp_f32 v128, v128\n"
      "v_exp_f32 v129, v129\n"
      "v_smfmac_f32_32x32x32_bf16 v[136:151], v[48:51], v[120:127], v156\n"
      "v_exp_f32 v130, v130\n"
      "v_exp_f32 v131, v131\n"
      "v_exp_f32 v132, v132\n"
      "v_exp_f32 v133, v133\n"
      "v_exp_f32 v134, v134\n"
      "v_exp_f32 v135, v135\n"
      "v_exp_f32 v104, v104\n"
      "v_exp_f32 v105, v105\n"
      "v_smfmac_f32_32x32x32_bf16 v[136:151], v[52:55], v[128:135], v156\n"
      "s_waitcnt vmcnt(10)\n"
      "v_exp_f32 v106, v106\n"
      "v_exp_f32 v107, v107\n"
      "v_exp_f32 v108, v108\n"
      "v_mfma_f32_32x32x16_bf16 v[120:135], v[20:23], v[152:155], -4.0\n"
      "s_waitcnt vmcnt(9)\n"
      "v_exp_f32 v109, v109\n"
      "v_exp_f32 v110, v110\n"
      "v_exp_f32 v111, v111\n"
      "v_exp_f32 v112, v112\n"
      "v_exp_f32 v113, v113\n"
      "v_smfmac_f32_32x32x32_bf16 v[136:151], v[56:59], v[104:111], v156\n"
      "v_exp_f32 v114, v114\n"
      "v_exp_f32 v115, v115\n"
      "v_exp_f32 v116, v116\n"
      "v_exp_f32 v117, v117\n"
      "v_exp_f32 v118, v118\n"
      "v_exp_f32 v119, v119\n"
      "v_exp_f32 v120, v120\n"
      "v_exp_f32 v121, v121\n"
      "v_smfmac_f32_32x32x32_bf16 v[136:151], v[60:63], v[112:119], v156\n"
      "s_waitcnt vmcnt(8)\n"
      "v_exp_f32 v122, v122\n"
      "v_exp_f32 v123, v123\n"
      "v_exp_f32 v124, v124\n"
      "v_mfma_f32_32x32x16_bf16 v[104:119], v[24:27], v[152:155], -4.0\n"
      "s_waitcnt vmcnt(7)\n"
      "v_exp_f32 v125, v125\n"
      "v_exp_f32 v126, v126\n"
      "v_exp_f32 v127, v127\n"
      "v_exp_f32 v128, v128\n"
      "v_exp_f32 v129, v129\n"
      "v_smfmac_f32_32x32x32_bf16 v[136:151], v[64:67], v[120:127], v156\n"
      "v_exp_f32 v130, v130\n"
      "v_exp_f32 v131, v131\n"
      "v_exp_f32 v132, v132\n"
      "v_exp_f32 v133, v133\n"
      "v_exp_f32 v134, v134\n"
      "v_exp_f32 v135, v135\n"
      "v_exp_f32 v104, v104\n"
      "v_exp_f32 v105, v105\n"
      "v_smfmac_f32_32x32x32_bf16 v[136:151], v[68:71], v[128:135], v156\n"
      "s_waitcnt vmcnt(6)\n"
      "v_exp_f32 v106, v106\n"
      "v_exp_f32 v107, v107\n"
      "v_exp_f32 v108, v108\n"
      "v_mfma_f32_32x32x16_bf16 v[120:135], v[28:31], v[152:155], -4.0\n"
      "s_waitcnt vmcnt(5)\n"
      "v_exp_f32 v109, v109\n"
      "v_exp_f32 v110, v110\n"
      "v_exp_f32 v111, v111\n"
      "v_exp_f32 v112, v112\n"
      "v_exp_f32 v113, v113\n"
      "v_smfmac_f32_32x32x32_bf16 v[136:151], v[72:75], v[104:111], v156\n"
      "v_exp_f32 v114, v114\n"
      "v_exp_f32 v115, v115\n"
      "v_exp_f32 v116, v116\n"
      "v_exp_f32 v117, v117\n"
      "v_exp_f32 v118, v118\n"
      "v_exp_f32 v119, v119\n"
      "v_exp_f32 v120, v120\n"
      "v_exp_f32 v121, v121\n"
      "v_smfmac_f32_32x32x32_bf16 v[136:151], v[76:79], v[112:119], v156\n"
      "s_waitcnt vmcnt(4)\n"
      "v_exp_f32 v122, v122\n"
      "v_exp_f32 v123, v123\n"
      "v_exp_f32 v124, v124\n"
      "v_mfma_f32_32x32x16_bf16 v[104:119], v[32:35], v[152:155], -4.0\n"
      "s_waitcnt vmcnt(3)\n"
      "v_exp_f32 v125, v125\n"
      "v_exp_f32 v126, v126\n"
      "v_exp_f32 v127, v127\n"
      "v_exp_f32 v128, v128\n"
      "v_exp_f32 v129, v129\n"
      "v_smfmac_f32_32x32x32_bf16 v[136:151], v[80:83], v[120:127], v156\n"
      "v_exp_f32 v130, v130\n"
      "v_exp_f32 v131, v131\n"
      "v_exp_f32 v132, v132\n"
      "v_exp_f32 v133, v133\n"
      "v_exp_f32 v134, v134\n"
      "v_exp_f32 v135, v135\n"
      "v_exp_f32 v104, v104\n"
      "v_exp_f32 v105, v105\n"
      "v_smfmac_f32_32x32x32_bf16 v[136:151], v[84:87], v[128:135], v156\n"
      "s_waitcnt vmcnt(2)\n"
      "v_exp_f32 v106, v106\n"
      "v_exp_f32 v107, v107\n"
      "v_exp_f32 v108, v108\n"
      "v_mfma_f32_32x32x16_bf16 v[120:135], v[36:39], v[152:155], -4.0\n"
      "ds_read_b128 v[152:155], v160 offset:2048\n"
      "s_waitcnt vmcnt(1)\n"
      "v_exp_f32 v109, v109\n"
      "v_exp_f32 v110, v110\n"
      "v_exp_f32 v111, v111\n"
      "v_exp_f32 v112, v112\n"
      "v_exp_f32 v113, v113\n"
      "v_smfmac_f32_32x32x32_bf16 v[136:151], v[88:91], v[104:111], v156\n"
      "v_exp_f32 v114, v114\n"
      "v_exp_f32 v115, v115\n"
      "v_exp_f32 v116, v116\n"
      "v_exp_f32 v117, v117\n"
      "v_exp_f32 v118, v118\n"
      "v_exp_f32 v119, v119\n"
      "v_exp_f32 v120, v120\n"
      "v_exp_f32 v121, v121\n"
      "v_smfmac_f32_32x32x32_bf16 v[136:151], v[92:95], v[112:119], v156\n"
      "s_waitcnt lgkmcnt(0)\n"
      "v_exp_f32 v122, v122\n"
      "v_exp_f32 v123, v123\n"
      "v_exp_f32 v124, v124\n"
      "v_mfma_f32_32x32x16_bf16 v[104:119], v[8:11], v[152:155], -4.0\n"
      "s_waitcnt vmcnt(0)\n"
      "v_exp_f32 v125, v125\n"
      "v_exp_f32 v126, v126\n"
      "v_exp_f32 v127, v127\n"
      "v_exp_f32 v128, v128\n"
      "v_exp_f32 v129, v129\n"
      "v_smfmac_f32_32x32x32_bf16 v[136:151], v[96:99], v[120:127], v156\n"
      "v_exp_f32 v130, v130\n"
      "v_exp_f32 v131, v131\n"
      "v_exp_f32 v132, v132\n"
      "v_exp_f32 v133, v133\n"
      "v_exp_f32 v134, v134\n"
      "v_exp_f32 v135, v135\n"
      "v_exp_f32 v104, v104\n"
      "v_exp_f32 v105, v105\n"
      "v_smfmac_f32_32x32x32_bf16 v[136:151], v[100:103], v[128:135], v156\n"
      "s_mul_i32 s42, s41, 2304\n"
      "s_add_u32 s42, s42, s67\n"
      "v_exp_f32 v106, v106\n"
      "v_exp_f32 v107, v107\n"
      "v_exp_f32 v108, v108\n"
      "v_mfma_f32_32x32x16_bf16 v[120:135], v[12:15], v[152:155], -4.0\n"
      "ds_read_b32 v159, v158 offset:152\n"
      "v_exp_f32 v109, v109\n"
      "v_exp_f32 v110, v110\n"
      "v_exp_f32 v111, v111\n"
      "v_exp_f32 v112, v112\n"
      "v_exp_f32 v113, v113\n"
      "v_exp_f32 v114, v114\n"
      "v_exp_f32 v115, v115\n"
      "v_exp_f32 v116, v116\n"
      "v_cmp_u_f32 vcc, v144, v144\n"
      "v_cmp_u_f32 s[78:79], v136, v137\n"
      "v_cmp_u_f32 s[80:81], v138, v139\n"
      "v_cmp_u_f32 s[82:83], v140, v141\n"
      "v_cmp_u_f32 s[84:85], v142, v143\n"
      "s_waitcnt lgkmcnt(0)\n"
      "v_cmp_gt_u32 s[52:53], 5, v159\n"
      "s_or_b64 vcc, vcc, s[78:79]\n"
      "s_or_b64 vcc, vcc, s[80:81]\n"
      "s_or_b64 vcc, vcc, s[82:83]\n"
      "s_or_b64 vcc, vcc, s[84:85]\n"
      "s_or_b64 s[52:53], vcc, s[52:53]\n"
      "s_cbranch_scc1 Lone%=\n"
      "Lclean%=:\n"
      "ds_write_b128 v161, v[136:139]\n"
      "ds_write_b128 v161, v[140:143] offset:1024\n"
      "ds_write_b32 v162, v144 offset:2048\n"
      "s_mov_b64 exec, 1\n"
      "ds_add_u32 v6, v157\n"
      "s_mov_b64 exec, -1\n"
      "v_add_u32 v161, s42, v2\n"
      "v_add_u32 v162, s42, v163\n"
      "ds_read_b128 v[136:139], v158 offset:32\n"
      "ds_read_b128 v[140:143], v158 offset:32\n"
      "ds_read_b128 v[144:147], v158 offset:32\n"
      "v_mov_b32 v3, s38\n"
      "s_branch Lrd%=\n"
      ".p2align 3\n"
      "Lstep%=:\n"
      "v_exp_f32 v104, v104\n"
      "v_exp_f32 v105, v105\n"
      "v_smfmac_f32_32x32x32_bf16 v[136:151], v[100:103], v[128:135], v156\n"
      "s_mul_i32 s42, s41, 2304\n"
      "s_add_u32 s42, s42, s67\n"
      "v_exp_f32 v106, v106\n"
      "v_exp_f32 v107, v107\n"
      "v_exp_f32 v108, v108\n"
      "v_exp_f32 v109, v109\n"
      "v_exp_f32 v110, v110\n"
      "v_exp_f32 v111, v111\n"
      "v_exp_f32 v112, v112\n"
      "v_exp_f32 v113, v113\n"
      "v_exp_f32 v114, v114\n"
      "v_exp_f32 v115, v115\n"
      "v_exp_f32 v116, v116\n"
      "v_mfma_f32_32x32x16_bf16 v[120:135], v[12:15], v[152:155], -4.0\n"
      "s_bitcmp1_b32 s68, s43\n"
      "s_cbranch_scc1 Lspec%=\n"
      "Lwr%=:\n"
      "ds_write_b128 v161, v[136:139]\n"
      "ds_write_b128 v161, v[140:143] offset:1024\n"
      "ds_write_b32 v162, v144 offset:2048\n"
      "s_mov_b64 exec, 1\n"
      "ds_add_u32 v6, v157\n"
      "s_mov_b64 exec, -1\n"
      "Lad%=:\n"
      "v_add_u32 v161, s42, v2\n"
      "v_add_u32 v162, s42, v163\n"
      "ds_read_b32 v159, v7\n"
      "ds_read_b128 v[136:139], v161\n"
      "ds_read_b128 v[140:143], v161 offset:1024\n"
      "ds_read_b32 v144, v162 offset:2048\n"
      "Lrd%=:\n"
      "v_exp_f32 v117, v117\n"
      "v_exp_f32 v118, v118\n"
      "v_exp_f32 v119, v119\n"
      "v_exp_f32 v120, v120\n"
      "v_exp_f32 v121, v121\n"
      "v_exp_f32 v122, v122\n"
      "v_exp_f32 v123, v123\n"
      "v_exp_f32 v124, v124\n"
      "s_sub_u32 s42, s43, 1\n"
      "s_waitcnt lgkmcnt(0)\n"
      "v_cmp_gt_i32 vcc, s42, v159\n"
      "s_cbranch_vccnz Lretry%=\n"
      "Lgo2%=:\n"
      "v_smfmac_f32_32x32x32_bf16 v[136:151], v[40:43], v[104:111], v156\n"
      "s_add_u32 s41, s41, 1\n"
      "s_and_b32 s41, s41, 7\n"
      "v_exp_f32 v125, v125\n"
      "v_exp_f32 v126, v126\n"
      "v_exp_f32 v127, v127\n"
      "v_smfmac_f32_32x32x32_bf16 v[136:151], v[44:47], v[112:119], v156\n"
      "s_lshl_b32 s50, s41, 10\n"
      "v_exp_f32 v128, v128\n"
      "v_exp_f32 v129, v129\n"
      "v_exp_f32 v130, v130\n"
      "v_mfma_f32_32x32x16_bf16 v[104:119], v[16:19], v[152:155], -4.0\n"
      "s_nop 0\n"
      "v_exp_f32 v131, v131\n"
      "v_exp_f32 v132, v132\n"
      "v_exp_f32 v133, v133\n"
      "v_smfmac_f32_32x32x32_bf16 v[136:151], v[48:51], v[120:127], v156\n"
      "s_nop 1\n"
      "v_exp_f32 v134, v134\n"
      "v_exp_f32 v135, v135\n"
      "v_add_u32 v160, s50, v2\n"
      "s_nop 1\n"
      "v_exp_f32 v104, v104\n"
      "v_exp_f32 v105, v105\n"
      "v_smfmac_f32_32x32x32_bf16 v[136:151], v[52:55], v[128:135], v156\n"
      "v_exp_f32 v106, v106\n"
      "v_exp_f32 v107, v107\n"
      "v_exp_f32 v108, v108\n"
      "v_mfma_f32_32x32x16_bf16 v[120:135], v[20:23], v[152:155], -4.0\n"
      "v_exp_f32 v109, v109\n"
      "v_exp_f32 v110, v110\n"
      "v_exp_f32 v111, v111\n"
      "v_exp_f32 v112, v112\n"
      "v_exp_f32 v113, v113\n"
      "v_smfmac_f32_32x32x32_bf16 v[136:151], v[56:59], v[104:111], v156\n"
      "v_exp_f32 v114, v114\n"
      "v_exp_f32 v115, v115\n"
      "v_exp_f32 v116, v116\n"
      "v_exp_f32 v117, v117\n"
      "v_exp_f32 v118, v118\n"
      "v_exp_f32 v119, v119\n"
      "v_exp_f32 v120, v120\n"
      "v_exp_f32 v121, v121\n"
      "v_smfmac_f32_32x32x32_bf16 v[136:151], v[60:63], v[112:119], v156\n"
      "v_exp_f32 v122, v122\n"
      "v_exp_f32 v123, v123\n"
      "v_exp_f32 v124, v124\n"
      "v_mfma_f32_32x32x16_bf16 v[104:119], v[24:27], v[152:155], -4.0\n"
      "v_exp_f32 v125, v125\n"
      "v_exp_f32 v126, v126\n"
      "v_exp_f32 v127, v127\n"
      "v_exp_f32 v128, v128\n"
      "v_exp_f32 v129, v129\n"
      "v_smfmac_f32_32x32x32_bf16 v[136:151], v[64:67], v[120:127], v156\n"
      "v_exp_f32 v130, v130\n"
      "v_exp_f32 v131, v131\n"
      "v_exp_f32 v132, v132\n"
      "v_exp_f32 v133, v133\n"
      "v_exp_f32 v134, v134\n"
      "v_exp_f32 v135, v135\n"
      "v_exp_f32 v104, v104\n"
      "v_exp_f32 v105, v105\n"
      "v_smfmac_f32_32x32x32_bf16 v[136:151], v[68:71], v[128:135], v156\n"
      "s_bitcmp1_b32 s69, s43\n"
      "v_exp_f32 v106, v106\n"
      "v_exp_f32 v107, v107\n"
      "v_exp_f32 v108, v108\n"
      "v_mfma_f32_32x32x16_bf16 v[120:135], v[28:31], v[152:155], -4.0\n"
      "v_exp_f32 v109, v109\n"
      "v_exp_f32 v110, v110\n"
      "v_exp_f32 v111, v111\n"
      "v_exp_f32 v112, v112\n"
      "v_exp_f32 v113, v113\n"
      "v_smfmac_f32_32x32x32_bf16 v[136:151], v[72:75], v[104:111], v156\n"
      "s_cbranch_scc1 Ldrop%=\n"
      "Ldropped%=:\n"
      "v_exp_f32 v114, v114\n"
      "v_exp_f32 v115, v115\n"
      "v_exp_f32 v116, v116\n"
      "v_exp_f32 v117, v117\n"
      "v_exp_f32 v118, v118\n"
      "v_exp_f32 v119, v119\n"
      "v_exp_f32 v120, v120\n"
      "v_exp_f32 v121, v121\n"
      "v_smfmac_f32_32x32x32_bf16 v[136:151], v[76:79], v[112:119], v156\n"
      "v_exp_f32 v122, v122\n"
      "v_exp_f32 v123, v123\n"
      "v_exp_f32 v124, v124\n"
      "v_mfma_f32_32x32x16_bf16 v[104:119], v[32:35], v[152:155], -4.0\n"
      "v_exp_f32 v125, v125\n"
      "v_exp_f32 v126, v126\n"
      "v_exp_f32 v127, v127\n"
      "v_exp_f32 v128, v128\n"
      "v_exp_f32 v129, v129\n"
      "v_smfmac_f32_32x32x32_bf16 v[136:151], v[80:83], v[120:127], v156\n"
      "v_exp_f32 v130, v130\n"
      "v_exp_f32 v131, v131\n"
      "v_exp_f32 v132, v132\n"
      "v_exp_f32 v133, v133\n"
      "v_exp_f32 v134, v134\n"
      "v_exp_f32 v135, v135\n"
      "v_exp_f32 v104, v104\n"
      "v_exp_f32 v105, v105\n"
      "v_smfmac_f32_32x32x32_bf16 v[136:151], v[84:87], v[128:135], v156\n"
      "v_exp_f32 v106, v106\n"
      "v_exp_f32 v107, v107\n"
      "v_exp_f32 v108, v108\n"
      "v_mfma_f32_32x32x16_bf16 v[120:135], v[36:39], v[152:155], -4.0\n"
      "ds_read_b128 v[152:155], v160 offset:1024\n"
      "v_exp_f32 v109, v109\n"
      "v_exp_f32 v110, v110\n"
      "v_exp_f32 v111, v111\n"
      "v_exp_f32 v112, v112\n"
      "v_exp_f32 v113, v113\n"
      "v_smfmac_f32_32x32x32_bf16 v[136:151], v[88:91], v[104:111], v156\n"
      "s_add_u32 s43, s43, 1\n"
      "v_exp_f32 v114, v114\n"
      "v_exp_f32 v115, v115\n"
      "v_exp_f32 v116, v116\n"
      "v_exp_f32 v117, v117\n"
      "v_exp_f32 v118, v118\n"
      "v_exp_f32 v119, v119\n"
      "v_exp_f32 v120, v120\n"
      "v_exp_f32 v121, v121\n"
      "v_smfmac_f32_32x32x32_bf16 v[136:151], v[92:95], v[112:119], v156\n"
      "s_waitcnt lgkmcnt(0)\n"
      "v_exp_f32 v122, v122\n"
      "v_exp_f32 v123, v123\n"
      "v_exp_f32 v124, v124\n"
      "v_mfma_f32_32x32x16_bf16 v[104:119], v[8:11], v[152:155], -4.0\n"
      "v_exp_f32 v125, v125\n"
      "v_exp_f32 v126, v126\n"
      "v_exp_f32 v127, v127\n"
      "v_exp_f32 v128, v128\n"
      "v_exp_f32 v129, v129\n"
      "v_smfmac_f32_32x32x32_bf16 v[136:151], v[96:99], v[120:127], v156\n"
      "s_cmp_lt_u32 s43, 8\n"
      "v_exp_f32 v130, v130\n"
      "v_exp_f32 v131, v131\n"
      "v_exp_f32 v132, v132\n"
      "v_exp_f32 v133, v133\n"
      "v_exp_f32 v134, v134\n"
      "v_exp_f32 v135, v135\n"
      "s_cbranch_scc1 Lstep%=\n"
      "s_setprio 3\n"
      "v_smfmac_f32_32x32x32_bf16 v[136:151], v[100:103], v[128:135], v156\n"
      "s_mov_b64 exec, 1\n"
      "buffer_atomic_add v3, off, s[16:19], s37 offset:512\n"
      "s_mov_b64 exec, -1\n"
      "s_nop 4\n"
      "Lfin%=:\n"
      "s_lshl_b32 s50, s43, 4\n"
      "s_lshl2_add_u32 s50, s61, s50\n"
      "v_mov_b32 v4, s50\n"
      "v_add_u32 v6, s57, v161\n"
      "v_add_u32 v164, s57, v162\n"
      "ds_write_b128 v161, v[136:139]\n"
      "ds_write_b128 v161, v[140:143] offset:1024\n"
      "ds_write_b32 v162, v144 offset:2048\n"
      "s_mov_b64 exec, 1\n"
      "ds_add_rtn_u32 v159, v4, v157 offset:48\n"
      "s_mov_b64 exec, -1\n"
      "ds_read_b32 v150, v158 offset:136\n"
      "ds_read_b32 v145, v164 offset:2048\n"
      "ds_read_b128 v[164:167], v6\n"
      "ds_read_b128 v[146:149], v6 offset:1024\n"
      "s_waitcnt lgkmcnt(3)\n"
      "v_readfirstlane_b32 s50, v159\n"
      "v_cmp_gt_u32 vcc, 4, v150\n"
      "s_cbranch_vccnz Lwo%=\n"
      "Lfok%=:\n"
      "s_bitcmp0_b32 s50, 0\n"
      "s_cbranch_scc1 Lfirst%=\n"
      "s_and_b32 %[bad], s50, 0x80\n"
      "s_waitcnt lgkmcnt(0)\n"
      "v_add_f32 v144, v144, v145\n"
      "v_pk_add_f32 v[136:137], v[136:137], v[164:165]\n"
      "v_rcp_f32 v150, v144\n"
      "v_pk_add_f32 v[138:139], v[138:139], v[166:167]\n"
      "v_pk_add_f32 v[140:141], v[140:141], v[146:147]\n"
      "v_pk_add_f32 v[142:143], v[142:143], v[148:149]\n"
      "v_pk_mul_f32 v[136:137], v[136:137], v[150:151] op_sel_hi:[1,0]\n"
      "v_pk_mul_f32 v[138:139], v[138:139], v[150:151] op_sel_hi:[1,0]\n"
      "v_pk_mul_f32 v[140:141], v[140:141], v[150:151] op_sel_hi:[1,0]\n"
      "v_pk_mul_f32 v[142:143], v[142:143], v[150:151] op_sel_hi:[1,0]\n"
      "v_pk_add_f32 v[146:147], v[136:137], v[138:139]\n"
      "v_pk_add_f32 v[148:149], v[140:141], v[142:143]\n"
      "s_mov_b32 s50, 0x2b800000\n"
      "v_cmp_le_f32 s[52:53], s50, v144\n"
      "v_pk_add_f32 v[146:147], v[146:147], v[148:149]\n"
      "s_mov_b32 s50, 0x67800000\n"
      "v_cmp_ge_f32 s[54:55], s50, v144\n"
      "v_add_f32 v146, v146, v147\n"
      "s_movk_i32 s50, 0x1f8\n"
      "v_cmp_class_f32 vcc, v146, s50\n"
      "v_cvt_pk_bf16_f32 v164, v136, v137\n"
      "v_cvt_pk_bf16_f32 v165, v138, v139\n"
      "v_cvt_pk_bf16_f32 v166, v140, v141\n"
      "v_cvt_pk_bf16_f32 v167, v142, v143\n"
      "s_and_b64 s[52:53], s[52:53], s[54:55]\n"
      "s_and_b64 vcc, vcc, s[52:53]\n"
      "s_cmp_eq_u64 vcc, -1\n"
      "s_cbranch_scc0 Lunsafe%=\n"
      "buffer_store_dwordx4 v[164:167], v2, s[16:19], s56 offen sc1\n"
      "Lpub%=:\n"
      "s_cmp_eq_u32 s43, 8\n"
      "s_cbranch_scc0 Lnext0%=\n"
      "s_mov_b64 exec, 1\n"
      "ds_add_u32 v158, v157 offset:140\n"
      "s_mov_b64 exec, -1\n"
      "s_cmp_lg_u32 %[bad], 0\n"
      "s_cbranch_scc1 Lout%=\n"
      "s_endpgm\n"
      "Lnext0%=:\n"
      "Lfirst%=:\n"
      "s_cmp_eq_u32 s43, 8\n"
      "s_cbranch_scc1 LA3%=\n"
      "Lnext%=:\n"
      "s_add_u32 s56, s56, 2048\n"
      "s_bitcmp1_b32 s68, 8\n"
      "s_cbranch_scc0 Lad%=\n"
      "s_sub_u32 s58, s58, 1\n"
      "s_setprio 1\n"
      "s_cmp_eq_u32 s58, 1\n"
      "s_cbranch_scc1 Lad%=\n"
      "s_setprio 0\n"
      "s_branch Lad%=\n"
      "Lunsafe%=:\n"
      "s_bitset1_b32 %[bad], s43\n"
      "v_mov_b32 v159, 0x80\n"
      "s_mov_b64 exec, 1\n"
      "ds_add_u32 v4, v159 offset:64\n"
      "s_mov_b64 exec, -1\n"
      "s_branch Lpub%=\n"
      "Lwo%=:\n"
      "s_mov_b32 s40, 200000\n"
      "Lwo2%=:\n"
      "ds_read_b32 v159, v158 offset:136\n"
      "s_waitcnt lgkmcnt(0)\n"
      "v_cmp_gt_u32 vcc, 4, v159\n"
      "s_sub_u32 s40, s40, 1\n"
      "s_cbranch_vccz Lwo2ok%=\n"
      "s_sleep 2\n"
      "s_cbranch_scc0 Lwo2%=\n"
      "Lwo2ok%=:\n"
      "s_branch Lfok%=\n"
      "Ldrop%=:\n"
      "s_sub_u32 s58, s58, 1\n"
      "s_setprio 1\n"
      "s_cmp_eq_u32 s58, 1\n"
      "s_cbranch_scc1 Ldropped%=\n"
      "s_setprio 0\n"
      "s_branch Ldropped%=\n"
      "Lspec%=:\n"
      "s_cmp_eq_u32 s43, 7\n"
      "s_cbranch_scc1 Lfin%=\n"
      "s_sub_u32 s58, s58, 1\n"
      "s_setprio 1\n"
      "s_cmp_eq_u32 s58, 1\n"
      "s_cbranch_scc1 Lwr%=\n"
      "s_setprio 0\n"
      "s_branch Lwr%=\n"
      "Lone%=:\n"
      "s_cbranch_vccz Lcr0%=\n"
      "s_cmp_eq_u32 s51, 0\n"
      "s_cbranch_scc1 Lverify%=\n"
      "Lcr0%=:\n"
      "s_mov_b32 s40, 200000\n"
      "Lcr%=:\n"
      "ds_read_b32 v159, v158 offset:152\n"
      "s_waitcnt lgkmcnt(0)\n"
      "v_cmp_gt_u32 vcc, 5, v159\n"
      "s_sub_u32 s40, s40, 1\n"
      "s_cbranch_vccz Lcrok%=\n"
      "s_sleep 2\n"
      "s_cbranch_scc0 Lcr%=\n"
      "Lcrok%=:\n"
      "s_branch Lclean%=\n"
      "Lretry%=:\n"
      "s_mov_b32 s40, 200000\n"
      "Lre%=:\n"
      "s_sleep 1\n"
      "ds_read_b32 v159, v7\n"
      "ds_read_b128 v[136:139], v161\n"
      "ds_read_b128 v[140:143], v161 offset:1024\n"
      "ds_read_b32 v144, v162 offset:2048\n"
      "s_waitcnt lgkmcnt(0)\n"
      "v_cmp_gt_i32 vcc, s42, v159\n"
      "s_sub_u32 s40, s40, 1\n"
      "s_cbranch_vccz Lgo2%=\n"
      "s_cbranch_scc0 Lre%=\n"
      "s_branch Lgo2%=\n"
      "Lverify%=:\n"
      "s_mov_b32 s51, 1\n"
      "v_and_b32 v4, 11, v1\n"
      "v_cmp_eq_u32 vcc, 0, v4\n"
      "v_mov_b32 v4, 0x3f803f80\n"
      "s_nop 0\n"
      "v_cndmask_b32 v40, 0, v4, vcc\n"
      "v_cndmask_b32 v41, 0, v4, vcc\n"
      "v_mov_b64 v[42:43], v[40:41]\n"
      "v_mov_b64 v[44:45], v[40:41]\n"
      "v_mov_b64 v[46:47], v[40:41]\n"
      "v_mov_b64 v[48:49], v[40:41]\n"
      "v_mov_b64 v[50:51], v[40:41]\n"
      "v_mov_b64 v[52:53], v[40:41]\n"
      "v_mov_b64 v[54:55], v[40:41]\n"
      "v_mov_b64 v[56:57], v[40:41]\n"
      "v_mov_b64 v[58:59], v[40:41]\n"
      "v_mov_b64 v[60:61], v[40:41]\n"
      "v_mov_b64 v[62:63], v[40:41]\n"
      "v_mov_b64 v[64:65], v[40:41]\n"
      "v_mov_b64 v[66:67], v[40:41]\n"
      "v_mov_b64 v[68:69], v[40:41]\n"
      "v_mov_b64 v[70:71], v[40:41]\n"
      "v_mov_b64 v[72:73], v[40:41]\n"
      "v_mov_b64 v[74:75], v[40:41]\n"
      "v_mov_b64 v[76:77], v[40:41]\n"
      "v_mov_b64 v[78:79], v[40:41]\n"
      "v_mov_b64 v[80:81], v[40:41]\n"
      "v_mov_b64 v[82:83], v[40:41]\n"
      "v_mov_b64 v[84:85], v[40:41]\n"
      "v_mov_b64 v[86:87], v[40:41]\n"
      "v_mov_b64 v[88:89], v[40:41]\n"
      "v_mov_b64 v[90:91], v[40:41]\n"
      "v_mov_b64 v[92:93], v[40:41]\n"
      "v_mov_b64 v[94:95], v[40:41]\n"
      "v_mov_b64 v[96:97], v[40:41]\n"
      "v_mov_b64 v[98:99], v[40:41]\n"
      "v_mov_b64 v[100:101], v[40:41]\n"
      "v_mov_b64 v[102:103], v[40:41]\n"
      "s_mov_b32 s40, 200000\n"
      "s_mov_b32 s50, s46\n"
      "s_add_u32 s42, s46, 16384\n"
      "Lv%=:\n"
      "buffer_load_dwordx4 v[164:167], v2, s[16:19], s50 offen sc1\n"
      "s_waitcnt vmcnt(0)\n"
      "v_max3_u32 v164, v164, v165, v166\n"
      "v_max_u32 v164, v164, v167\n"
      "v_cmp_eq_u32 vcc, -1, v164\n"
      "s_sub_u32 s40, s40, 1\n"
      "s_cbranch_scc1 Lagain%=\n"
      "s_cbranch_vccnz Lv%=\n"
      "s_add_u32 s50, s50, 1024\n"
      "s_cmp_lt_u32 s50, s42\n"
      "s_cbranch_scc1 Lv%=\n"
      "s_branch Lagain%=\n"
      "LA3%=:\n"
      "s_lshr_b32 s42, s61, 1\n"
      "s_and_b32 s43, s61, 1\n"
      "s_lshl1_add_u32 s43, s63, s43\n"
      "s_lshl1_add_u32 s46, s43, s42\n"
      "s_lshl_b32 s46, s46, 14\n"
      "s_add_u32 s46, s46, 1048576\n"
      "s_add_u32 s47, s46, 4096\n"
      "s_add_u32 s48, s46, 8192\n"
      "s_add_u32 s49, s46, 12288\n"
      "v_xor_b32 v3, 64, v0\n"
      "v_mov_b32 v6, 0\n"
      "s_and_b32 s50, s61, 1\n"
      "s_lshl1_add_u32 s50, s50, s42\n"
      "s_lshl3_add_u32 s50, s63, s50\n"
      "s_lshl_b32 s50, s50, 7\n"
      "s_add_u32 s37, s50, 32768\n"
      "buffer_load_dwordx4 v[104:107], v2, s[16:19], s46 offen offset:0 sc1\n"
      "buffer_load_dwordx4 v[108:111], v2, s[16:19], s46 offen offset:1024 sc1\n"
      "buffer_load_dwordx4 v[112:115], v2, s[16:19], s47 offen offset:0 sc1\n"
      "buffer_load_dwordx4 v[116:119], v2, s[16:19], s47 offen offset:1024 sc1\n"
      "buffer_load_dwordx4 v[120:123], v2, s[16:19], s48 offen offset:0 sc1\n"
      "buffer_load_dwordx4 v[124:127], v2, s[16:19], s48 offen offset:1024 sc1\n"
      "buffer_load_dwordx4 v[128:131], v2, s[16:19], s49 offen offset:0 sc1\n"
      "buffer_load_dwordx4 v[132:135], v2, s[16:19], s49 offen offset:1024 sc1\n"
      "ds_read_b64_tr_b16 v[72:73], v3 offset:0\n"
      "ds_read_b64_tr_b16 v[74:75], v3 offset:1024\n"
      "ds_read_b64_tr_b16 v[40:41], v0 offset:0\n"
      "ds_read_b64_tr_b16 v[42:43], v0 offset:1024\n"
      "ds_read_b64_tr_b16 v[76:77], v3 offset:2048\n"
      "ds_read_b64_tr_b16 v[78:79], v3 offset:3072\n"
      "ds_read_b64_tr_b16 v[44:45], v0 offset:2048\n"
      "ds_read_b64_tr_b16 v[46:47], v0 offset:3072\n"
      "ds_read_b64_tr_b16 v[80:81], v3 offset:8192\n"
      "ds_read_b64_tr_b16 v[82:83], v3 offset:9216\n"
      "ds_read_b64_tr_b16 v[48:49], v0 offset:8192\n"
      "ds_read_b64_tr_b16 v[50:51], v0 offset:9216\n"
      "ds_read_b64_tr_b16 v[84:85], v3 offset:10240\n"
      "ds_read_b64_tr_b16 v[86:87], v3 offset:11264\n"
      "ds_read_b64_tr_b16 v[52:53], v0 offset:10240\n"
      "ds_read_b64_tr_b16 v[54:55], v0 offset:11264\n"
      "ds_read_b64_tr_b16 v[88:89], v3 offset:16384\n"
      "ds_read_b64_tr_b16 v[90:91], v3 offset:17408\n"
      "ds_read_b64_tr_b16 v[56:57], v0 offset:16384\n"
      "ds_read_b64_tr_b16 v[58:59], v0 offset:17408\n"
      "ds_read_b64_tr_b16 v[92:93], v3 offset:18432\n"
      "ds_read_b64_tr_b16 v[94:95], v3 offset:19456\n"
      "ds_read_b64_tr_b16 v[60:61], v0 offset:18432\n"
      "ds_read_b64_tr_b16 v[62:63], v0 offset:19456\n"
      "ds_read_b64_tr_b16 v[96:97], v3 offset:24576\n"
      "ds_read_b64_tr_b16 v[98:99], v3 offset:25600\n"
      "ds_read_b64_tr_b16 v[64:65], v0 offset:24576\n"
      "ds_read_b64_tr_b16 v[66:67], v0 offset:25600\n"
      "ds_read_b64_tr_b16 v[100:101], v3 offset:26624\n"
      "ds_read_b64_tr_b16 v[102:103], v3 offset:27648\n"
      "ds_read_b64_tr_b16 v[68:69], v0 offset:26624\n"
      "ds_read_b64_tr_b16 v[70:71], v0 offset:27648\n"
      "s_lshl_b32 s56, s43, 17\n"
      "s_lshl1_add_u32 s42, s62, s42\n"
      "s_lshl_b32 s42, s42, 7\n"
      "s_add_u32 s56, s56, s42\n"
      "s_lshl_b32 s42, s61, 13\n"
      "s_add_u32 s43, s42, 9216\n"
      "s_xor_b32 s42, s42, 0x4000\n"
      "s_add_u32 s42, s42, 13312\n"
      "s_mov_b32 s40, 200000\n"
      "Lfn%=:\n"
      "ds_read_b32 v159, v4 offset:48\n"
      "s_waitcnt lgkmcnt(0)\n"
      "v_cmp_gt_u32 vcc, 2, v159\n"
      "s_sub_u32 s40, s40, 1\n"
      "s_cbranch_vccz Lfnok%=\n"
      "s_sleep 1\n"
      "s_cbranch_scc0 Lfn%=\n"
      "Lfnok%=:\n"
      "s_setprio 2\n"
      "s_mov_b32 s51, 0\n"
      "s_mov_b32 s40, 200000\n"
      "v_mov_b32 v7, 0\n"
      "s_mov_b64 exec, 1\n"
      "buffer_atomic_add v7, off, s[16:19], s37 offset:512 sc0\n"
      "s_mov_b64 exec, -1\n"
      "s_waitcnt vmcnt(1)\n"
      "v_max3_u32 v4, v104, v105, v106\n"
      "v_max3_u32 v24, v107, v108, v109\n"
      "v_max3_u32 v4, v4, v110, v111\n"
      "v_max3_u32 v24, v24, v112, v113\n"
      "v_max3_u32 v4, v4, v114, v115\n"
      "v_max3_u32 v24, v24, v116, v117\n"
      "v_max3_u32 v4, v4, v118, v119\n"
      "v_max3_u32 v24, v24, v120, v121\n"
      "v_max3_u32 v4, v4, v122, v123\n"
      "v_max3_u32 v24, v24, v124, v125\n"
      "v_max3_u32 v4, v4, v126, v127\n"
      "v_max3_u32 v24, v24, v128, v129\n"
      "v_max3_u32 v4, v4, v130, v131\n"
      "v_max3_u32 v24, v24, v132, v133\n"
      "v_max3_u32 v4, v4, v134, v135\n"
      "v_max_u32 v4, v4, v24\n"
      "v_cmp_eq_u32 vcc, -1, v4\n"
      "s_cbranch_vccnz Lr00%=\n"
      "Lk00%=:\n"
      "v_mfma_f32_32x32x16_bf16 v[8:23], v[72:75], v[104:107], 0\n"
      "ds_read_b64_tr_b16 v[72:73], v3 offset:4096\n"
      "ds_read_b64_tr_b16 v[74:75], v3 offset:5120\n"
      "v_mfma_f32_32x32x16_bf16 v[136:151], v[40:43], v[104:107], 0\n"
      "ds_read_b64_tr_b16 v[40:41], v0 offset:4096\n"
      "ds_read_b64_tr_b16 v[42:43], v0 offset:5120\n"
      "v_mfma_f32_32x32x16_bf16 v[8:23], v[76:79], v[108:111], v[8:23]\n"
      "ds_read_b64_tr_b16 v[76:77], v3 offset:6144\n"
      "ds_read_b64_tr_b16 v[78:79], v3 offset:7168\n"
      "v_mfma_f32_32x32x16_bf16 v[136:151], v[44:47], v[108:111], v[136:151]\n"
      "ds_read_b64_tr_b16 v[44:45], v0 offset:6144\n"
      "ds_read_b64_tr_b16 v[46:47], v0 offset:7168\n"
      "v_mfma_f32_32x32x16_bf16 v[8:23], v[80:83], v[112:115], v[8:23]\n"
      "ds_read_b64_tr_b16 v[80:81], v3 offset:12288\n"
      "ds_read_b64_tr_b16 v[82:83], v3 offset:13312\n"
      "v_mfma_f32_32x32x16_bf16 v[136:151], v[48:51], v[112:115], v[136:151]\n"
      "ds_read_b64_tr_b16 v[48:49], v0 offset:12288\n"
      "ds_read_b64_tr_b16 v[50:51], v0 offset:13312\n"
      "v_mfma_f32_32x32x16_bf16 v[8:23], v[84:87], v[116:119], v[8:23]\n"
      "ds_read_b64_tr_b16 v[84:85], v3 offset:14336\n"
      "ds_read_b64_tr_b16 v[86:87], v3 offset:15360\n"
      "v_mfma_f32_32x32x16_bf16 v[136:151], v[52:55], v[116:119], v[136:151]\n"
      "ds_read_b64_tr_b16 v[52:53], v0 offset:14336\n"
      "ds_read_b64_tr_b16 v[54:55], v0 offset:15360\n"
      "v_mfma_f32_32x32x16_bf16 v[8:23], v[88:91], v[120:123], v[8:23]\n"
      "ds_read_b64_tr_b16 v[88:89], v3 offset:20480\n"
      "ds_read_b64_tr_b16 v[90:91], v3 offset:21504\n"
      "v_mfma_f32_32x32x16_bf16 v[136:151], v[56:59], v[120:123], v[136:151]\n"
      "ds_read_b64_tr_b16 v[56:57], v0 offset:20480\n"
      "ds_read_b64_tr_b16 v[58:59], v0 offset:21504\n"
      "v_mfma_f32_32x32x16_bf16 v[8:23], v[92:95], v[124:127], v[8:23]\n"
      "ds_read_b64_tr_b16 v[92:93], v3 offset:22528\n"
      "ds_read_b64_tr_b16 v[94:95], v3 offset:23552\n"
      "v_mfma_f32_32x32x16_bf16 v[136:151], v[60:63], v[124:127], v[136:151]\n"
      "ds_read_b64_tr_b16 v[60:61], v0 offset:22528\n"
      "ds_read_b64_tr_b16 v[62:63], v0 offset:23552\n"
      "v_mfma_f32_32x32x16_bf16 v[8:23], v[96:99], v[128:131], v[8:23]\n"
      "ds_read_b64_tr_b16 v[96:97], v3 offset:28672\n"
      "ds_read_b64_tr_b16 v[98:99], v3 offset:29696\n"
      "v_mfma_f32_32x32x16_bf16 v[136:151], v[64:67], v[128:131], v[136:151]\n"
      "ds_read_b64_tr_b16 v[64:65], v0 offset:28672\n"
      "ds_read_b64_tr_b16 v[66:67], v0 offset:29696\n"
      "v_mfma_f32_32x32x16_bf16 v[8:23], v[100:103], v[132:135], v[8:23]\n"
      "ds_read_b64_tr_b16 v[100:101], v3 offset:30720\n"
      "ds_read_b64_tr_b16 v[102:103], v3 offset:31744\n"
      "v_mfma_f32_32x32x16_bf16 v[136:151], v[68:71], v[132:135], v[136:151]\n"
      "ds_read_b64_tr_b16 v[68:69], v0 offset:30720\n"
      "ds_read_b64_tr_b16 v[70:71], v0 offset:31744\n"
      "s_lshl_b32 s50, s61, 2\n"
      "v_mov_b32 v116, s50\n"
      "s_waitcnt vmcnt(0)\n"
      "s_branch Lg1w%=\n"
      "Lg1%=:\n"
      "v_mov_b32 v7, 0\n"
      "s_mov_b64 exec, 1\n"
      "buffer_atomic_add v7, off, s[16:19], s37 offset:512 sc0\n"
      "s_mov_b64 exec, -1\n"
      "s_waitcnt vmcnt(0)\n"
      "Lg1w%=:\n"
      "v_readfirstlane_b32 s50, v7\n"
      "s_sub_u32 s40, s40, 1\n"
      "s_cselect_b32 s50, -1, s50\n"
      "s_and_b32 s50, s50, 67372036\n"
      "s_andn2_b32 s52, s50, s51\n"
      "s_cbranch_scc0 Lg1%=\n"
      "s_bitcmp1_b32 s52, 2\n"
      "s_cbranch_scc0 Lg10%=\n"
      "buffer_load_dwordx4 v[24:27], v2, s[16:19], s46 offen offset:2048 sc1\n"
      "buffer_load_dwordx4 v[28:31], v2, s[16:19], s46 offen offset:3072 sc1\n"
      "Lg10%=:\n"
      "s_bitcmp1_b32 s52, 10\n"
      "s_cbranch_scc0 Lg11%=\n"
      "buffer_load_dwordx4 v[32:35], v2, s[16:19], s47 offen offset:2048 sc1\n"
      "buffer_load_dwordx4 v[36:39], v2, s[16:19], s47 offen offset:3072 sc1\n"
      "Lg11%=:\n"
      "s_bitcmp1_b32 s52, 18\n"
      "s_cbranch_scc0 Lg12%=\n"
      "buffer_load_dwordx4 v[152:155], v2, s[16:19], s48 offen offset:2048 sc1\n"
      "buffer_load_dwordx4 v[156:159], v2, s[16:19], s48 offen offset:3072 sc1\n"
      "Lg12%=:\n"
      "s_bitcmp1_b32 s52, 26\n"
      "s_cbranch_scc0 Lg13%=\n"
      "buffer_load_dwordx4 v[160:163], v2, s[16:19], s49 offen offset:2048 sc1\n"
      "buffer_load_dwordx4 v[164:167], v2, s[16:19], s49 offen offset:3072 sc1\n"
      "Lg13%=:\n"
      "s_or_b32 s51, s51, s50\n"
      "s_cmp_lg_u32 s51, 67372036\n"
      "s_cbranch_scc1 Lg1%=\n"
      "v_lshrrev_b32 v111, 5, v1\n"
      "v_xor_b32 v111, v111, v1\n"
      "v_and_b32 v111, 7, v111\n"
      "v_lshlrev_b32 v111, 4, v111\n"
      "v_and_b32 v112, 31, v1\n"
      "v_lshl_or_b32 v111, v112, 7, v111\n"
      "v_add_u32 v104, s43, v111\n"
      "v_xor_b32 v105, 32, v104\n"
      "v_xor_b32 v106, 64, v104\n"
      "v_xor_b32 v107, 96, v104\n"
      "v_lshrrev_b32 v112, 3, v1\n"
      "v_lshlrev_b32 v111, 4, v112\n"
      "v_xor_b32 v111, v111, v2\n"
      "v_add_u32 v108, s43, v111\n"
      "v_add_u32 v109, s42, v111\n"
      "v_and_b32 v111, 0x70, v2\n"
      "v_lshl_or_b32 v110, v112, 12, v111\n"
      "v_mov_b32 v7, 1\n"
      "s_mov_b32 s40, 200000\n"
      "s_waitcnt vmcnt(0) lgkmcnt(0)\n"
      "v_max3_u32 v4, v24, v25, v26\n"
      "v_max3_u32 v113, v27, v28, v29\n"
      "v_max3_u32 v4, v4, v30, v31\n"
      "v_max3_u32 v113, v113, v32, v33\n"
      "v_max3_u32 v4, v4, v34, v35\n"
      "v_max3_u32 v113, v113, v36, v37\n"
      "v_max3_u32 v4, v4, v38, v39\n"
      "v_max_u32 v4, v4, v113\n"
      "v_cmp_eq_u32 vcc, -1, v4\n"
      "s_cbranch_vccnz Lr10%=\n"
      "Lk10%=:\n"
      "v_mfma_f32_32x32x16_bf16 v[8:23], v[72:75], v[24:27], v[8:23]\n"
      "v_mfma_f32_32x32x16_bf16 v[136:151], v[40:43], v[24:27], v[136:151]\n"
      "v_mfma_f32_32x32x16_bf16 v[8:23], v[76:79], v[28:31], v[8:23]\n"
      "v_mfma_f32_32x32x16_bf16 v[136:151], v[44:47], v[28:31], v[136:151]\n"
      "v_mfma_f32_32x32x16_bf16 v[8:23], v[80:83], v[32:35], v[8:23]\n"
      "s_waitcnt vmcnt(0)\n"
      "v_max3_u32 v4, v152, v153, v154\n"
      "v_max3_u32 v113, v155, v156, v157\n"
      "v_max3_u32 v4, v4, v158, v159\n"
      "v_mfma_f32_32x32x16_bf16 v[136:151], v[48:51], v[32:35], v[136:151]\n"
      "v_max3_u32 v113, v113, v160, v161\n"
      "v_max3_u32 v4, v4, v162, v163\n"
      "v_mfma_f32_32x32x16_bf16 v[8:23], v[84:87], v[36:39], v[8:23]\n"
      "v_max3_u32 v113, v113, v164, v165\n"
      "v_max3_u32 v4, v4, v166, v167\n"
      "v_max_u32 v4, v4, v113\n"
      "v_mfma_f32_32x32x16_bf16 v[136:151], v[52:55], v[36:39], v[136:151]\n"
      "v_cmp_eq_u32 vcc, -1, v4\n"
      "s_cbranch_vccnz Lr14%=\n"
      "Lk14%=:\n"
      "v_mfma_f32_32x32x16_bf16 v[8:23], v[88:91], v[152:155], v[8:23]\n"
      "ds_read_b32 v115, v6 offset:140\n"
      "v_mfma_f32_32x32x16_bf16 v[8:23], v[92:95], v[156:159], v[8:23]\n"
      "v_mfma_f32_32x32x16_bf16 v[8:23], v[96:99], v[160:163], v[8:23]\n"
      "s_waitcnt lgkmcnt(0)\n"
      "v_cmp_gt_u32 vcc, 4, v115\n"
      "v_mfma_f32_32x32x16_bf16 v[8:23], v[100:103], v[164:167], v[8:23]\n"
      "s_cbranch_vccnz Lhw%=\n"
      "Lhb%=:\n"
      "v_mfma_f32_32x32x16_bf16 v[136:151], v[56:59], v[152:155], v[136:151]\n"
      "s_nop 7\n"
      "s_nop 2\n"
      "ds_write_b128 v104, v[8:11] offset:4096\n"
      "ds_write_b128 v105, v[12:15] offset:4096\n"
      "v_mfma_f32_32x32x16_bf16 v[136:151], v[60:63], v[156:159], v[136:151]\n"
      "ds_write_b128 v106, v[16:19] offset:4096\n"
      "ds_write_b128 v107, v[20:23] offset:4096\n"
      "s_mov_b64 exec, 1\n"
      "ds_add_u32 v6, v7 offset:144\n"
      "s_mov_b64 exec, -1\n"
      "v_mfma_f32_32x32x16_bf16 v[136:151], v[64:67], v[160:163], v[136:151]\n"
      "v_mfma_f32_32x32x16_bf16 v[136:151], v[68:71], v[164:167], v[136:151]\n"
      "s_mov_b32 s40, 200000\n"
      "s_nop 7\n"
      "s_nop 2\n"
      "ds_write_b128 v104, v[136:139]\n"
      "ds_write_b128 v105, v[140:143]\n"
      "ds_write_b128 v106, v[144:147]\n"
      "ds_write_b128 v107, v[148:151]\n"
      "ds_read_b128 v[56:59], v108 offset:0\n"
      "ds_read_b128 v[60:63], v108 offset:1024\n"
      "ds_read_b128 v[64:67], v108 offset:2048\n"
      "ds_read_b128 v[68:71], v108 offset:3072\n"
      "Lab%=:\n"
      "ds_read_b32 v4, v6 offset:144\n"
      "ds_read_b128 v[40:43], v109 offset:0\n"
      "ds_read_b128 v[44:47], v109 offset:1024\n"
      "ds_read_b128 v[48:51], v109 offset:2048\n"
      "ds_read_b128 v[52:55], v109 offset:3072\n"
      "s_waitcnt lgkmcnt(4)\n"
      "v_cmp_gt_u32 vcc, 4, v4\n"
      "s_sub_u32 s40, s40, 1\n"
      "s_cbranch_vccz Labok%=\n"
      "s_cbranch_scc0 Lab%=\n"
      "Labok%=:\n"
      "s_waitcnt lgkmcnt(3)\n"
      "v_pk_add_f32 v[40:41], v[40:41], v[56:57]\n"
      "v_pk_add_f32 v[42:43], v[42:43], v[58:59]\n"
      "buffer_store_dwordx4 v[40:43], v110, s[20:23], s56 offen sc1\n"
      "s_add_u32 s56, s56, 0x8000\n"
      "s_waitcnt lgkmcnt(2)\n"
      "v_pk_add_f32 v[44:45], v[44:45], v[60:61]\n"
      "v_pk_add_f32 v[46:47], v[46:47], v[62:63]\n"
      "buffer_store_dwordx4 v[44:47], v110, s[20:23], s56 offen sc1\n"
      "s_add_u32 s56, s56, 0x8000\n"
      "s_waitcnt lgkmcnt(1)\n"
      "v_pk_add_f32 v[48:49], v[48:49], v[64:65]\n"
      "v_pk_add_f32 v[50:51], v[50:51], v[66:67]\n"
      "buffer_store_dwordx4 v[48:51], v110, s[20:23], s56 offen sc1\n"
      "s_add_u32 s56, s56, 0x8000\n"
      "s_waitcnt lgkmcnt(0)\n"
      "v_pk_add_f32 v[52:53], v[52:53], v[68:69]\n"
      "v_pk_add_f32 v[54:55], v[54:55], v[70:71]\n"
      "buffer_store_dwordx4 v[52:55], v110, s[20:23], s56 offen sc1\n"
      "s_endpgm\n"
      "Lr00%=:\n"
      "s_sleep 4\n"
      "buffer_load_dwordx4 v[104:107], v2, s[16:19], s46 offen offset:0 sc1\n"
      "buffer_load_dwordx4 v[108:111], v2, s[16:19], s46 offen offset:1024 sc1\n"
      "buffer_load_dwordx4 v[112:115], v2, s[16:19], s47 offen offset:0 sc1\n"
      "buffer_load_dwordx4 v[116:119], v2, s[16:19], s47 offen offset:1024 sc1\n"
      "buffer_load_dwordx4 v[120:123], v2, s[16:19], s48 offen offset:0 sc1\n"
      "buffer_load_dwordx4 v[124:127], v2, s[16:19], s48 offen offset:1024 sc1\n"
      "buffer_load_dwordx4 v[128:131], v2, s[16:19], s49 offen offset:0 sc1\n"
      "buffer_load_dwordx4 v[132:135], v2, s[16:19], s49 offen offset:1024 sc1\n"
      "s_waitcnt vmcnt(0)\n"
      "v_max3_u32 v4, v104, v105, v106\n"
      "v_max3_u32 v24, v107, v108, v109\n"
      "v_max3_u32 v4, v4, v110, v111\n"
      "v_max3_u32 v24, v24, v112, v113\n"
      "v_max3_u32 v4, v4, v114, v115\n"
      "v_max3_u32 v24, v24, v116, v117\n"
      "v_max3_u32 v4, v4, v118, v119\n"
      "v_max3_u32 v24, v24, v120, v121\n"
      "v_max3_u32 v4, v4, v122, v123\n"
      "v_max3_u32 v24, v24, v124, v125\n"
      "v_max3_u32 v4, v4, v126, v127\n"
      "v_max3_u32 v24, v24, v128, v129\n"
      "v_max3_u32 v4, v4, v130, v131\n"
      "v_max3_u32 v24, v24, v132, v133\n"
      "v_max3_u32 v4, v4, v134, v135\n"
      "v_max_u32 v4, v4, v24\n"
      "v_cmp_eq_u32 vcc, -1, v4\n"
      "s_sub_u32 s40, s40, 1\n"
      "s_cbranch_vccz Lk00%=\n"
      "s_cbranch_scc0 Lr00%=\n"
      "s_mov_b32 s40, 0\n"
      "s_branch Lk00%=\n"
      "Lr10%=:\n"
      "s_sleep 6\n"
      "buffer_load_dwordx4 v[24:27], v2, s[16:19], s46 offen offset:2048 sc1\n"
      "buffer_load_dwordx4 v[28:31], v2, s[16:19], s46 offen offset:3072 sc1\n"
      "buffer_load_dwordx4 v[32:35], v2, s[16:19], s47 offen offset:2048 sc1\n"
      "buffer_load_dwordx4 v[36:39], v2, s[16:19], s47 offen offset:3072 sc1\n"
      "buffer_load_dwordx4 v[152:155], v2, s[16:19], s48 offen offset:2048 sc1\n"
      "buffer_load_dwordx4 v[156:159], v2, s[16:19], s48 offen offset:3072 sc1\n"
      "buffer_load_dwordx4 v[160:163], v2, s[16:19], s49 offen offset:2048 sc1\n"
      "buffer_load_dwordx4 v[164:167], v2, s[16:19], s49 offen offset:3072 sc1\n"
      "s_waitcnt vmcnt(0)\n"
      "v_max3_u32 v4, v24, v25, v26\n"
      "v_max3_u32 v113, v27, v28, v29\n"
      "v_max3_u32 v4, v4, v30, v31\n"
      "v_max3_u32 v113, v113, v32, v33\n"
      "v_max3_u32 v4, v4, v34, v35\n"
      "v_max3_u32 v113, v113, v36, v37\n"
      "v_max3_u32 v4, v4, v38, v39\n"
      "v_max_u32 v4, v4, v113\n"
      "v_cmp_eq_u32 vcc, -1, v4\n"
      "s_sub_u32 s40, s40, 1\n"
      "s_cbranch_vccz Lk10%=\n"
      "s_cbranch_scc0 Lr10%=\n"
      "s_mov_b32 s40, 0\n"
      "s_branch Lk10%=\n"
      "Lr14%=:\n"
      "s_sleep 6\n"
      "buffer_load_dwordx4 v[152:155], v2, s[16:19], s48 offen offset:2048 sc1\n"
      "buffer_load_dwordx4 v[156:159], v2, s[16:19], s48 offen offset:3072 sc1\n"
      "buffer_load_dwordx4 v[160:163], v2, s[16:19], s49 offen offset:2048 sc1\n"
      "buffer_load_dwordx4 v[164:167], v2, s[16:19], s49 offen offset:3072 sc1\n"
      "s_waitcnt vmcnt(0)\n"
      "v_max3_u32 v4, v152, v153, v154\n"
      "v_max3_u32 v113, v155, v156, v157\n"
      "v_max3_u32 v4, v4, v158, v159\n"
      "v_max3_u32 v113, v113, v160, v161\n"
      "v_max3_u32 v4, v4, v162, v163\n"
      "v_max3_u32 v113, v113, v164, v165\n"
      "v_max3_u32 v4, v4, v166, v167\n"
      "v_max_u32 v4, v4, v113\n"
      "v_cmp_eq_u32 vcc, -1, v4\n"
      "s_sub_u32 s40, s40, 1\n"
      "s_cbranch_vccz Lk14%=\n"
      "s_cbranch_scc0 Lr14%=\n"
      "s_mov_b32 s40, 0\n"
      "s_branch Lk14%=\n"
      "Lhw%=:\n"
      "ds_read_b32 v115, v6 offset:140\n"
      "s_waitcnt lgkmcnt(0)\n"
      "v_cmp_gt_u32 vcc, 4, v115\n"
      "s_sub_u32 s40, s40, 1\n"
      "s_cbranch_vccz Lhb%=\n"
      "s_cbranch_scc0 Lhw%=\n"
      "s_branch Lhb%=\n"
      "Lout%=:\n"
      "s_mov_b32 %[wave], s60\n"
      : [wave] "=&s"(wave), [bad] "=&s"(bad), [ka] "+s"(ka), [tid] "+v"(tid), "={v152}"(raw_lr), "={v153}"(raw_li), "={v154}"(raw_dt), "={v155}"(raw_dk)
      : [bid] "s"(b), "s"((unsigned)(size_t)(LAS char*)smem)
      :
"v1", "v2", "v3", "v4", "v5", "v6", "v7", "v8", "v9", "v10", "v11", "v12", "v13", "v14", "v15", "v16", "v17", "v18", "v19", "v20", "v21", "v22", "v23", "v24", "v25", "v26", "v27", "v28", "v29", "v30", "v31", "v32", "v33", "v34", "v35", "v36", "v37", "v38", "v39", "v40", "v41", "v42", "v43", "v44", "v45", "v46", "v47", "v48", "v49", "v50", "v51", "v52", "v53", "v54", "v55", "v56", "v57", "v58", "v59", "v60", "v61", "v62", "v63", "v64", "v65", "v66", "v67", "v68", "v69", "v70", "v71", "v72", "v73", "v74", "v75", "v76", "v77", "v78", "v79", "v80", "v81", "v82", "v83", "v84", "v85", "v86", "v87", "v88", "v89", "v90", "v91", "v92", "v93", "v94", "v95", "v96", "v97", "v98", "v99", "v100", "v101", "v102", "v103", "v104", "v105", "v106", "v107", "v108", "v109", "v110", "v111", "v112", "v113", "v114", "v115", "v116", "v117", "v118", "v119", "v120", "v121", "v122", "v123", "v124", "v125", "v126", "v127", "v128", "v129", "v130", "v131", "v132", "v133", "v134", "v135", "v136", "v137", "v138", "v139", "v140", "v141", "v142", "v143", "v144", "v145", "v146", "v147", "v148", "v149", "v150", "v151", "v156", "v157", "v158", "v159", "v160", "v161", "v162", "v163", "v164", "v165", "v166", "v167", "s8", "s9", "s10", "s11", "s12", "s13", "s14", "s15", "s16", "s17", "s18", "s19", "s20", "s21", "s22", "s23", "s24", "s25", "s26", "s27", "s28", "s29", "s30", "s31", "s32", "s33", "s34", "s35", "s36", "s37", "s38", "s39", "s40", "s41", "s42", "s43", "s44", "s45", "s46", "s47", "s48", "s49", "s50", "s51", "s52", "s53", "s54", "s55", "s56", "s57", "s58", "s59", "s60", "s61", "s62", "s63", "s64", "s65", "s66", "s67", "s68", "s69", "s70", "s71", "s72", "s73", "s74", "s75", "s76", "s77", "s78", "s79", "s80", "s81", "s82", "s83", "s84", "s85", "s86", "s87", "s88", "s89", "vcc", "scc", "memory"
  );
  const unsigned tb = (b & 7) * 4 + (b >> 6), j = (b >> 3) & 7;
  const unsigned s = wave & 3;
  if (__builtin_expect(bad != 0, 0)) {
    const rsrc_t rws = mk(ka->ws.base, W_END);
    const unsigned qe = (6 + 2 * s) & 7;
    const unsigned ofe = W_O + ((2 * tb + (qe >> 2)) * 32 + 4 * j + ((qe >> 1) & 1)) * 1024;
#pragma unroll 1
    for (unsigned ph = 0; ph < 2; ++ph)
      if ((bad >> (7 + ph)) & 1) exact_tile(rws, qe + ph, ofe + ph * 2048);
    return;
  }

  LANE_VALUES
  unsigned l16 = lane * 16; PIN(l16);
  const unsigned tq = (lane & 15) >> 2, tp = lane & 3, blk = (lane >> 4) & 1;
  if (s == 3) {
    __builtin_amdgcn_s_setprio(2);
    const float dt = bf2f(__builtin_bit_cast(u16, (__bf16)expf(bf2f(raw_dt)))), lr = bf2f(raw_lr), li = bf2f(raw_li);
    float sn, cs;
    sincosf(dt * li, &sn, &cs);
    const float e = expf(dt * lr);
    const f32x2 c1 = {e * cs, e * sn};
    const float d = 2.f / (lr * lr + li * li), nr = c1.x - 1.f;
    f32x2 t = cmul(c1, c1); t = cmul(t, t); t = cmul(t, t);
    const f32x2 c16v = cmul(t, t), c32 = cmul(c16v, c16v), c64 = cmul(c32, c32);
    t = cmul(c64, c64); t = cmul(t, t); t = cmul(t, t);
    f32x4 o0 = {c1.x, c1.y, (nr * lr + c1.y * li) * d, (c1.y * lr - nr * li) * d}, o1 = {c16v.x, c16v.y, c32.x, c32.y}, o2 = {c64.x, c64.y, t.x, t.y};
    PIN(o0); PIN(o1); PIN(o2);
    await<L_GO, 2>(y.zero, 1);
    *(LAS f32x4*)(L_CST + l16) = o0; *(LAS f32x4*)(L_CST + 1024 + l16) = o1; *(LAS f32x4*)(L_CST + 2048 + l16) = o2;
    arrive<L_CR>(y);
    __builtin_amdgcn_s_setprio(3);
  }
  const unsigned r1 = L_R1 + s * 12288;
  const unsigned dv = (r32 & 1) ? raw_dk << 16 : raw_dk, dd = (r32 >> 1) - 4 * h;
  u32x8 dg = {dd == 0 ? dv : 0u, dd == 1 ? dv : 0u, dd == 2 ? dv : 0u, dd == 3 ? dv : 0u, dd == 8 ? dv : 0u, dd == 9 ? dv : 0u, dd == 10 ? dv : 0u, dd == 11 ? dv : 0u};
  const unsigned x0 = L_R0 + r32 * 256 + ((h ^ c16) << 4), b0 = r1 + r32 * 128 + ((h ^ ((r32 >> 1) & 7)) << 4);
  u32x8 xa = {x0, x0 ^ 32, x0 ^ 64, x0 ^ 96, x0 ^ 128, x0 ^ 160, x0 ^ 192, x0 ^ 224};
  const unsigned vxb = (16 * (i4 & 1) + (i4 >> 1)) * 1024 + ((c16 ^ i4) << 4), vb0 = (lane >> 3) * 1024 + (((lane & 7) ^ i4) << 4);
  u32x8 m0 = {vxb, (vxb ^ 64) + 31 * 1024, vxb ^ 128, (vxb ^ 192) + 31 * 1024, (32 - tb + 8 * i4) * 128 + c16 * 8, lane < 16 ? lane * 8 : 0x7FFFFFF0u, (lane & 7) * 8,
              0u};
  u32x16 ps = {0u, l16, 0u, 1u, vb0, (vb0 ^ 64) + 7168, b0, b0 ^ 32, b0 ^ 64, b0 ^ 96, vw, c16 * 4, SENT, SENT, SENT, SENT};
  const unsigned tok = 32 * (s & 1) + r32;
  const unsigned slot = 32 * ((tok >> 3) & 1) + ((tok >> 4) & 1) + 2 * (tok & 1) + 4 * (tok >> 5) + 8 * ((tok >> 1) & 3);
  const unsigned xd = L_R0 + (j >> 1) * 16384 + slot * 256 + (((8 * (j & 1) + 4 * (s >> 1) + h) ^ (slot & 15)) << 4);
  u32x4 m1 = {xd, xd ^ 32, r1 + 8192 + c16 * 128 + i4 * 32, r1 + 8192 + (8 * h + tq) * 128 + blk * 32 + tp * 8};
  PIN(dg); PIN(xa); PIN(m0); PIN(ps); PIN(m1);
  await<L_CR, 2>(y.zero, 1);
  const unsigned ca = L_CST + s * 256 + c16 * 16;
  f32x4 k0 = *(LAS f32x4*)ca, k1 = *(LAS f32x4*)(ca + 1024), k2 = *(LAS f32x4*)(ca + 2048);
  arrive<L_CR>(y);
  k1.zw = cmul((i4 & 1) ? k1.xy : f32x2{1.f, 0.f}, (i4 & 2) ? k1.zw : f32x2{1.f, 0.f});
  asm volatile(
      ".p2align 3\n"
      "s_load_dwordx8 s[8:15], %[ka], 0x80\n"
      "s_load_dwordx4 s[16:19], %[ka], 0xa0\n"
      "s_load_dwordx8 s[20:27], %[ka], 0x20\n"
      "s_mul_i32 s66, %[s], 12288\n"
      "s_add_u32 s66, s66, 46080\n"
      "s_lshl_b32 s67, %[s], 12\n"
      "s_add_u32 s74, s67, 98304\n"
      "s_lshl_b32 s69, %[tb], 16\n"
      "s_add_u32 s69, s69, s67\n"
      "s_lshl_b32 s67, %[s], 14\n"
      "s_lshl_b32 s49, %[j], 16\n"
      "s_add_u32 s68, s49, s67\n"
      "s_add_u32 s67, s67, 98304\n"
      "s_lshr_b32 s50, %[s], 1\n"
      "s_lshl_b32 s70, s50, 15\n"
      "s_add_u32 s70, s70, s49\n"
      "s_lshl_b32 s51, %[s], 8\n"
      "s_add_u32 s70, s70, s51\n"
      "s_add_u32 s52, %[s], 3\n"
      "s_bfe_u32 s53, s52, 0x10001\n"
      "s_lshl1_add_u32 s53, %[tb], s53\n"
      "s_lshl3_add_u32 s53, s53, %[j]\n"
      "s_and_b32 s52, s52, 1\n"
      "s_lshl2_add_u32 s53, s53, s52\n"
      "s_lshl_b32 s53, s53, 10\n"
      "s_add_u32 s71, s53, 1048576\n"
      "s_lshl_b32 s72, %[s], 17\n"
      "s_lshl_b32 s52, %[j], 7\n"
      "s_add_u32 s72, s72, s52\n"
      "s_and_b32 s52, %[s], 1\n"
      "s_lshl1_add_u32 s52, %[tb], s52\n"
      "s_lshl3_add_u32 s73, s52, %[s]\n"
      "s_lshl_b32 s73, s73, 13\n"
      "s_add_u32 s73, s73, 3145728\n"
      "s_lshl_b32 s75, s52, 17\n"
      "s_lshl1_add_u32 s50, %[j], s50\n"
      "s_lshl_b32 s50, s50, 7\n"
      "s_add_u32 s75, s75, s50\n"
      "s_add_u32 s75, s75, 0x800\n"
      "s_getpc_b64 s[56:57]\n"
      "s_lshl2_add_u32 s77, %[s], 400\n"
      "v_and_b32 v104, 12, v163\n"
      "s_lshl3_add_u32 s78, %[tb], %[j]\n"
      "s_lshl_b32 s78, s78, 7\n"
      "s_add_u32 s78, s78, 32768\n"
      "s_lshl2_add_u32 s76, %[j], %[s]\n"
      "s_mov_b32 s60, 0xffff0000\n"
      "s_mov_b32 s61, 0\n"
      "s_mov_b32 s62, 0\n"
      "s_mov_b32 s63, 0xffff\n"
      "s_mov_b32 s64, 0\n"
      "s_mov_b32 s65, 0xffff0000\n"
      "s_waitcnt lgkmcnt(0)\n"
      "s_mov_b64 s[28:29], s[20:21]\n"
      "s_mov_b32 s30, 0x700000\n"
      "s_mov_b32 s31, 0x20000\n"
      "s_mov_b64 s[32:33], s[10:11]\n"
      "s_mov_b32 s34, 0x200000\n"
      "s_mov_b32 s35, 0x20000\n"
      "s_mov_b64 s[36:37], s[12:13]\n"
      "s_mov_b32 s38, 0x80000\n"
      "s_mov_b32 s39, 0x20000\n"
      "s_mov_b64 s[40:41], s[14:15]\n"
      "s_mov_b32 s42, 0x80000\n"
      "s_mov_b32 s43, 0x20000\n"
      "s_mov_b64 s[44:45], s[16:17]\n"
      "s_mov_b32 s46, 0x80000\n"
      "s_mov_b32 s47, 0x20000\n"
      "s_mov_b32 s48, 200000\n"
      "Lseen%=:\n"
      "ds_read_b32 v152, v154 offset:116\n"
      "s_waitcnt lgkmcnt(0)\n"
      "v_cmp_gt_u32 vcc, 8, v152\n"
      "s_sub_u32 s48, s48, 1\n"
      "s_cbranch_vccz Lseenok%=\n"
      "s_sleep 2\n"
      "s_cbranch_scc0 Lseen%=\n"
      "Lseenok%=:\n"
      "s_add_u32 m0, s66, 0\n"
      "s_add_u32 s49, s68, 0\n"
      "buffer_load_dwordx4 v156, s[36:39], s49 offen lds\n"
      "buffer_load_dwordx4 v157, s[36:39], s49 offen offset:1024 lds\n"
      "s_add_u32 m0, s66, 2048\n"
      "s_nop 0\n"
      "buffer_load_dwordx4 v156, s[40:43], s49 offen lds\n"
      "buffer_load_dwordx4 v157, s[40:43], s49 offen offset:1024 lds\n"
      "s_add_u32 m0, s74, 0\n"
      "s_add_u32 s49, s69, 0\n"
      "buffer_load_dwordx4 v144, s[32:35], s49 offen offset:0 lds\n"
      "buffer_load_dwordx4 v145, s[32:35], s49 offen offset:1024 lds\n"
      "buffer_load_dwordx4 v146, s[32:35], s49 offen offset:2048 lds\n"
      "buffer_load_dwordx4 v147, s[32:35], s49 offen offset:3072 lds\n"
      "s_add_u32 m0, s77, 0\n"
      "s_mov_b64 exec, 1\n"
      "global_load_lds_dword v154, s[56:57]\n"
      "s_mov_b64 exec, -1\n"
      "s_add_u32 m0, s66, 4096\n"
      "s_add_u32 s49, s68, 128\n"
      "buffer_load_dwordx4 v156, s[36:39], s49 offen lds\n"
      "buffer_load_dwordx4 v157, s[36:39], s49 offen offset:1024 lds\n"
      "s_add_u32 m0, s66, 6144\n"
      "s_nop 0\n"
      "buffer_load_dwordx4 v156, s[40:43], s49 offen lds\n"
      "buffer_load_dwordx4 v157, s[40:43], s49 offen offset:1024 lds\n"
      "s_add_u32 m0, s74, 16384\n"
      "s_add_u32 s49, s69, 256\n"
      "buffer_load_dwordx4 v144, s[32:35], s49 offen offset:0 lds\n"
      "buffer_load_dwordx4 v145, s[32:35], s49 offen offset:1024 lds\n"
      "buffer_load_dwordx4 v146, s[32:35], s49 offen offset:2048 lds\n"
      "buffer_load_dwordx4 v147, s[32:35], s49 offen offset:3072 lds\n"
      "s_add_u32 m0, s77, 16\n"
      "s_mov_b64 exec, 1\n"
      "global_load_lds_dword v154, s[56:57]\n"
      "s_mov_b64 exec, -1\n"
      "s_add_u32 m0, s66, 8192\n"
      "s_add_u32 s49, s68, 256\n"
      "buffer_load_dwordx4 v156, s[36:39], s49 offen lds\n"
      "buffer_load_dwordx4 v157, s[36:39], s49 offen offset:1024 lds\n"
      "s_add_u32 m0, s66, 10240\n"
      "s_nop 0\n"
      "buffer_load_dwordx4 v156, s[40:43], s49 offen lds\n"
      "buffer_load_dwordx4 v157, s[40:43], s49 offen offset:1024 lds\n"
      "s_waitcnt vmcnt(14)\n"
      "s_mov_b32 s48, 200000\n"
      "Lx0%=:\n"
      "ds_read_b32 v152, v104 offset:400\n"
      "s_waitcnt lgkmcnt(0)\n"
      "v_cmp_gt_u32 vcc, 1, v152\n"
      "s_sub_u32 s48, s48, 1\n"
      "s_cbranch_vccz Lx0ok%=\n"
      "s_sleep 1\n"
      "s_cbranch_scc0 Lx0%=\n"
      "Lx0ok%=:\n"
      "s_setprio 0\n"
      "ds_read_b128 v[48:51], v158 offset:0\n"
      "ds_read_b128 v[52:55], v159 offset:0\n"
      "ds_read_b128 v[56:59], v160 offset:0\n"
      "ds_read_b128 v[60:63], v161 offset:0\n"
      "ds_read_b128 v[64:67], v112 offset:0\n"
      "ds_read_b128 v[68:71], v112 offset:8192\n"
      "ds_read_b128 v[72:75], v113 offset:0\n"
      "ds_read_b128 v[76:79], v113 offset:8192\n"
      "ds_read_b128 v[80:83], v114 offset:0\n"
      "ds_read_b128 v[84:87], v114 offset:8192\n"
      "s_waitcnt lgkmcnt(4)\n"
      ".p2align 3\n"
      "v_mfma_f32_32x32x16_bf16 v[16:31], v[64:67], v[48:51], 0\n"
      "s_waitcnt vmcnt(9)\n"
      "ds_read_b128 v[88:91], v158 offset:4096\n"
      "ds_read_b128 v[64:67], v115 offset:0\n"
      "s_add_u32 m0, s66, 0\n"
      "s_add_u32 s49, s68, 384\n"
      "buffer_load_dwordx4 v156, s[36:39], s49 offen lds\n"
      ".p2align 3\n"
      "v_mfma_f32_32x32x16_bf16 v[32:47], v[68:71], v[48:51], 0\n"
      "ds_read_b128 v[92:95], v159 offset:4096\n"
      "ds_read_b128 v[68:71], v115 offset:8192\n"
      "buffer_load_dwordx4 v157, s[36:39], s49 offen offset:1024 lds\n"
      "s_add_u32 m0, s66, 2048\n"
      "s_waitcnt lgkmcnt(6)\n"
      ".p2align 3\n"
      "v_mfma_f32_32x32x16_bf16 v[16:31], v[72:75], v[52:55], v[16:31]\n"
      "ds_read_b128 v[96:99], v160 offset:4096\n"
      "ds_read_b128 v[72:75], v116 offset:0\n"
      "s_nop 0\n"
      "buffer_load_dwordx4 v156, s[40:43], s49 offen lds\n"
      ".p2align 3\n"
      "v_mfma_f32_32x32x16_bf16 v[32:47], v[76:79], v[52:55], v[32:47]\n"
      "ds_read_b128 v[100:103], v161 offset:4096\n"
      "ds_read_b128 v[76:79], v116 offset:8192\n"
      "buffer_load_dwordx4 v157, s[40:43], s49 offen offset:1024 lds\n"
      "s_waitcnt lgkmcnt(8)\n"
      ".p2align 3\n"
      "v_mfma_f32_32x32x16_bf16 v[16:31], v[80:83], v[56:59], v[16:31]\n"
      "s_waitcnt lgkmcnt(1)\n"
      "ds_read_b128 v[80:83], v117 offset:0\n"
      "s_add_u32 m0, s66, 4096\n"
      "s_add_u32 s49, s68, 512\n"
      "buffer_load_dwordx4 v156, s[36:39], s49 offen lds\n"
      ".p2align 3\n"
      "v_mfma_f32_32x32x16_bf16 v[32:47], v[84:87], v[56:59], v[32:47]\n"
      "ds_read_b128 v[84:87], v117 offset:8192\n"
      "buffer_load_dwordx4 v157, s[36:39], s49 offen offset:1024 lds\n"
      "s_add_u32 m0, s66, 6144\n"
      ".p2align 3\n"
      "v_mfma_f32_32x32x16_bf16 v[16:31], v[64:67], v[60:63], v[16:31]\n"
      "ds_read_b128 v[64:67], v118 offset:0\n"
      "s_nop 0\n"
      "buffer_load_dwordx4 v156, s[40:43], s49 offen lds\n"
      ".p2align 3\n"
      "v_mfma_f32_32x32x16_bf16 v[32:47], v[68:71], v[60:63], v[32:47]\n"
      "ds_read_b128 v[68:71], v118 offset:8192\n"
      "buffer_load_dwordx4 v157, s[40:43], s49 offen offset:1024 lds\n"
      "s_waitcnt lgkmcnt(4)\n"
      ".p2align 3\n"
      "v_mfma_f32_32x32x16_bf16 v[16:31], v[72:75], v[88:91], v[16:31]\n"
      "s_waitcnt vmcnt(8)\n"
      "ds_read_b128 v[48:51], v158 offset:8192\n"
      "ds_read_b128 v[72:75], v119 offset:0\n"
      "ds_read_b32 v152, v104 offset:416\n"
      "s_add_u32 m0, s74, 32768\n"
      "s_add_u32 s49, s69, 512\n"
      "buffer_load_dwordx4 v144, s[32:35], s49 offen offset:0 lds\n"
      ".p2align 3\n"
      "v_mfma_f32_32x32x16_bf16 v[32:47], v[76:79], v[88:91], v[32:47]\n"
      "ds_read_b128 v[52:55], v159 offset:8192\n"
      "ds_read_b128 v[76:79], v119 offset:8192\n"
      "buffer_load_dwordx4 v145, s[32:35], s49 offen offset:1024 lds\n"
      "s_waitcnt lgkmcnt(7)\n"
      ".p2align 3\n"
      "v_mfma_f32_32x32x16_bf16 v[16:31], v[80:83], v[92:95], v[16:31]\n"
      "ds_read_b128 v[56:59], v160 offset:8192\n"
      "s_waitcnt lgkmcnt(3)\n"
      "s_setprio 3\n"
      "v_cmp_gt_u32 vcc, 1, v152\n"
      "s_cbranch_vccz Lx1ok%=\n"
      "s_mov_b32 s48, 200000\n"
      "Lx1%=:\n"
      "ds_read_b32 v152, v104 offset:416\n"
      "s_waitcnt lgkmcnt(0)\n"
      "v_cmp_gt_u32 vcc, 1, v152\n"
      "s_sub_u32 s48, s48, 1\n"
      "s_cbranch_vccz Lx1ok%=\n"
      "s_sleep 1\n"
      "s_cbranch_scc0 Lx1%=\n"
      "Lx1ok%=:\n"
      "s_setprio 0\n"
      "ds_read_b128 v[80:83], v112 offset:16384\n"
      "buffer_load_dwordx4 v146, s[32:35], s49 offen offset:2048 lds\n"
      ".p2align 3\n"
      "v_mfma_f32_32x32x16_bf16 v[32:47], v[84:87], v[92:95], v[32:47]\n"
      "ds_read_b128 v[60:63], v161 offset:8192\n"
      "ds_read_b128 v[84:87], v112 offset:24576\n"
      "buffer_load_dwordx4 v147, s[32:35], s49 offen offset:3072 lds\n"
      "s_add_u32 m0, s77, 32\n"
      "s_mov_b64 exec, 1\n"
      "global_load_lds_dword v154, s[56:57]\n"
      "s_mov_b64 exec, -1\n"
      ".p2align 3\n"
      "v_mfma_f32_32x32x16_bf16 v[16:31], v[64:67], v[96:99], v[16:31]\n"
      "s_waitcnt lgkmcnt(1)\n"
      "ds_read_b128 v[64:67], v113 offset:16384\n"
      "s_add_u32 m0, s66, 8192\n"
      "s_add_u32 s49, s68, 640\n"
      "buffer_load_dwordx4 v156, s[36:39], s49 offen lds\n"
      ".p2align 3\n"
      "v_mfma_f32_32x32x16_bf16 v[32:47], v[68:71], v[96:99], v[32:47]\n"
      "ds_read_b128 v[68:71], v113 offset:24576\n"
      "buffer_load_dwordx4 v157, s[36:39], s49 offen offset:1024 lds\n"
      "s_add_u32 m0, s66, 10240\n"
      ".p2align 3\n"
      "v_mfma_f32_32x32x16_bf16 v[16:31], v[72:75], v[100:103], v[16:31]\n"
      "ds_read_b128 v[72:75], v114 offset:16384\n"
      "s_nop 0\n"
      "buffer_load_dwordx4 v156, s[40:43], s49 offen lds\n"
      ".p2align 3\n"
      "v_mfma_f32_32x32x16_bf16 v[32:47], v[76:79], v[100:103], v[32:47]\n"
      "ds_read_b128 v[76:79], v114 offset:24576\n"
      "buffer_load_dwordx4 v157, s[40:43], s49 offen offset:1024 lds\n"
      "s_waitcnt lgkmcnt(4)\n"
      ".p2align 3\n"
      "v_mfma_f32_32x32x16_bf16 v[16:31], v[80:83], v[48:51], v[16:31]\n"
      "s_waitcnt vmcnt(13)\n"
      "ds_read_b128 v[88:91], v158 offset:0\n"
      "ds_read_b128 v[80:83], v115 offset:16384\n"
      "s_add_u32 m0, s74, 49152\n"
      "s_add_u32 s49, s69, 768\n"
      "buffer_load_dwordx4 v144, s[32:35], s49 offen offset:0 lds\n"
      ".p2align 3\n"
      "v_mfma_f32_32x32x16_bf16 v[32:47], v[84:87], v[48:51], v[32:47]\n"
      "ds_read_b128 v[92:95], v159 offset:0\n"
      "ds_read_b128 v[84:87], v115 offset:24576\n"
      "buffer_load_dwordx4 v145, s[32:35], s49 offen offset:1024 lds\n"
      "s_waitcnt lgkmcnt(6)\n"
      ".p2align 3\n"
      "v_mfma_f32_32x32x16_bf16 v[16:31], v[64:67], v[52:55], v[16:31]\n"
      "ds_read_b128 v[96:99], v160 offset:0\n"
      "ds_read_b128 v[64:67], v116 offset:16384\n"
      "buffer_load_dwordx4 v146, s[32:35], s49 offen offset:2048 lds\n"
      ".p2align 3\n"
      "v_mfma_f32_32x32x16_bf16 v[32:47], v[68:71], v[52:55], v[32:47]\n"
      "ds_read_b128 v[100:103], v161 offset:0\n"
      "ds_read_b128 v[68:71], v116 offset:24576\n"
      "buffer_load_dwordx4 v147, s[32:35], s49 offen offset:3072 lds\n"
      "s_add_u32 m0, s77, 48\n"
      "s_mov_b64 exec, 1\n"
      "global_load_lds_dword v154, s[56:57]\n"
      "s_mov_b64 exec, -1\n"
      "s_waitcnt lgkmcnt(8)\n"
      ".p2align 3\n"
      "v_mfma_f32_32x32x16_bf16 v[16:31], v[72:75], v[56:59], v[16:31]\n"
      "s_waitcnt lgkmcnt(1)\n"
      "ds_read_b128 v[72:75], v117 offset:16384\n"
      "s_add_u32 m0, s66, 0\n"
      "s_add_u32 s49, s68, 768\n"
      "buffer_load_dwordx4 v156, s[36:39], s49 offen lds\n"
      ".p2align 3\n"
      "v_mfma_f32_32x32x16_bf16 v[32:47], v[76:79], v[56:59], v[32:47]\n"
      "ds_read_b128 v[76:79], v117 offset:24576\n"
      "buffer_load_dwordx4 v157, s[36:39], s49 offen offset:1024 lds\n"
      "s_add_u32 m0, s66, 2048\n"
      ".p2align 3\n"
      "v_mfma_f32_32x32x16_bf16 v[16:31], v[80:83], v[60:63], v[16:31]\n"
      "ds_read_b128 v[80:83], v118 offset:16384\n"
      "s_nop 0\n"
      "buffer_load_dwordx4 v156, s[40:43], s49 offen lds\n"
      ".p2align 3\n"
      "v_mfma_f32_32x32x16_bf16 v[32:47], v[84:87], v[60:63], v[32:47]\n"
      "ds_read_b128 v[84:87], v118 offset:24576\n"
      "buffer_load_dwordx4 v157, s[40:43], s49 offen offset:1024 lds\n"
      "s_waitcnt lgkmcnt(4)\n"
      ".p2align 3\n"
      "v_mfma_f32_32x32x16_bf16 v[16:31], v[64:67], v[88:91], v[16:31]\n"
      "s_waitcnt vmcnt(18)\n"
      "ds_read_b128 v[48:51], v158 offset:4096\n"
      "ds_read_b128 v[64:67], v119 offset:16384\n"
      "ds_read_b32 v152, v104 offset:432\n"
      ".p2align 3\n"
      "v_mfma_f32_32x32x16_bf16 v[32:47], v[68:71], v[88:91], v[32:47]\n"
      "ds_read_b128 v[52:55], v159 offset:4096\n"
      "ds_read_b128 v[68:71], v119 offset:24576\n"
      "s_waitcnt lgkmcnt(7)\n"
      ".p2align 3\n"
      "v_mfma_f32_32x32x16_bf16 v[16:31], v[72:75], v[92:95], v[16:31]\n"
      "ds_read_b128 v[56:59], v160 offset:4096\n"
      "s_waitcnt lgkmcnt(3)\n"
      "s_setprio 3\n"
      "v_cmp_gt_u32 vcc, 1, v152\n"
      "s_cbranch_vccz Lx2ok%=\n"
      "s_mov_b32 s48, 200000\n"
      "Lx2%=:\n"
      "ds_read_b32 v152, v104 offset:432\n"
      "s_waitcnt lgkmcnt(0)\n"
      "v_cmp_gt_u32 vcc, 1, v152\n"
      "s_sub_u32 s48, s48, 1\n"
      "s_cbranch_vccz Lx2ok%=\n"
      "s_sleep 1\n"
      "s_cbranch_scc0 Lx2%=\n"
      "Lx2ok%=:\n"
      "s_setprio 0\n"
      "ds_read_b128 v[72:75], v112 offset:32768\n"
      ".p2align 3\n"
      "v_mfma_f32_32x32x16_bf16 v[32:47], v[76:79], v[92:95], v[32:47]\n"
      "ds_read_b128 v[60:63], v161 offset:4096\n"
      "ds_read_b128 v[76:79], v112 offset:40960\n"
      ".p2align 3\n"
      "v_mfma_f32_32x32x16_bf16 v[16:31], v[80:83], v[96:99], v[16:31]\n"
      "s_waitcnt lgkmcnt(1)\n"
      "ds_read_b128 v[80:83], v113 offset:32768\n"
      "s_add_u32 m0, s66, 4096\n"
      "s_add_u32 s49, s68, 896\n"
      "buffer_load_dwordx4 v156, s[36:39], s49 offen lds\n"
      ".p2align 3\n"
      "v_mfma_f32_32x32x16_bf16 v[32:47], v[84:87], v[96:99], v[32:47]\n"
      "ds_read_b128 v[84:87], v113 offset:40960\n"
      "buffer_load_dwordx4 v157, s[36:39], s49 offen offset:1024 lds\n"
      "s_add_u32 m0, s66, 6144\n"
      ".p2align 3\n"
      "v_mfma_f32_32x32x16_bf16 v[16:31], v[64:67], v[100:103], v[16:31]\n"
      "ds_read_b128 v[64:67], v114 offset:32768\n"
      "s_nop 0\n"
      "buffer_load_dwordx4 v156, s[40:43], s49 offen lds\n"
      ".p2align 3\n"
      "v_mfma_f32_32x32x16_bf16 v[32:47], v[68:71], v[100:103], v[32:47]\n"
      "ds_read_b128 v[68:71], v114 offset:40960\n"
      "buffer_load_dwordx4 v157, s[40:43], s49 offen offset:1024 lds\n"
      "s_waitcnt lgkmcnt(4)\n"
      ".p2align 3\n"
      "v_mfma_f32_32x32x16_bf16 v[16:31], v[72:75], v[48:51], v[16:31]\n"
      "s_waitcnt vmcnt(13)\n"
      "ds_read_b128 v[88:91], v158 offset:8192\n"
      "ds_read_b128 v[72:75], v115 offset:32768\n"
      ".p2align 3\n"
      "v_mfma_f32_32x32x16_bf16 v[32:47], v[76:79], v[48:51], v[32:47]\n"
      "ds_read_b128 v[92:95], v159 offset:8192\n"
      "ds_read_b128 v[76:79], v115 offset:40960\n"
      "s_waitcnt lgkmcnt(6)\n"
      ".p2align 3\n"
      "v_mfma_f32_32x32x16_bf16 v[16:31], v[80:83], v[52:55], v[16:31]\n"
      "ds_read_b128 v[96:99], v160 offset:8192\n"
      "ds_read_b128 v[80:83], v116 offset:32768\n"
      ".p2align 3\n"
      "v_mfma_f32_32x32x16_bf16 v[32:47], v[84:87], v[52:55], v[32:47]\n"
      "ds_read_b128 v[100:103], v161 offset:8192\n"
      "ds_read_b128 v[84:87], v116 offset:40960\n"
      "s_waitcnt lgkmcnt(8)\n"
      ".p2align 3\n"
      "v_mfma_f32_32x32x16_bf16 v[16:31], v[64:67], v[56:59], v[16:31]\n"
      "ds_read_b128 v[64:67], v117 offset:32768\n"
      "buffer_store_dwordx4 v[164:167], v153, s[28:31], s71 offen sc1\n"
      ".p2align 3\n"
      "v_mfma_f32_32x32x16_bf16 v[32:47], v[68:71], v[56:59], v[32:47]\n"
      "ds_read_b128 v[68:71], v117 offset:40960\n"
      "buffer_store_dwordx4 v[164:167], v153, s[28:31], s71 offen offset:2048 sc1\n"
      "s_waitcnt lgkmcnt(6)\n"
      ".p2align 3\n"
      "v_mfma_f32_32x32x16_bf16 v[16:31], v[72:75], v[60:63], v[16:31]\n"
      "ds_read_b128 v[72:75], v118 offset:32768\n"
      "s_mov_b64 exec, 1\n"
      "buffer_store_dword v154, off, s[28:31], s78 sc1\n"
      "s_mov_b64 exec, -1\n"
      ".p2align 3\n"
      "v_mfma_f32_32x32x16_bf16 v[32:47], v[76:79], v[60:63], v[32:47]\n"
      "ds_read_b128 v[76:79], v118 offset:40960\n"
      "s_waitcnt lgkmcnt(4)\n"
      ".p2align 3\n"
      "v_mfma_f32_32x32x16_bf16 v[16:31], v[80:83], v[88:91], v[16:31]\n"
      "s_waitcnt vmcnt(7)\n"
      "ds_read_b128 v[48:51], v158 offset:0\n"
      "ds_read_b128 v[80:83], v119 offset:32768\n"
      "ds_read_b32 v152, v104 offset:448\n"
      ".p2align 3\n"
      "v_mfma_f32_32x32x16_bf16 v[32:47], v[84:87], v[88:91], v[32:47]\n"
      "ds_read_b128 v[52:55], v159 offset:0\n"
      "ds_read_b128 v[84:87], v119 offset:40960\n"
      "s_waitcnt lgkmcnt(7)\n"
      ".p2align 3\n"
      "v_mfma_f32_32x32x16_bf16 v[16:31], v[64:67], v[92:95], v[16:31]\n"
      "ds_read_b128 v[56:59], v160 offset:0\n"
      "s_waitcnt lgkmcnt(3)\n"
      "s_setprio 3\n"
      "v_cmp_gt_u32 vcc, 1, v152\n"
      "s_cbranch_vccz Lx3ok%=\n"
      "s_mov_b32 s48, 200000\n"
      "Lx3%=:\n"
      "ds_read_b32 v152, v104 offset:448\n"
      "s_waitcnt lgkmcnt(0)\n"
      "v_cmp_gt_u32 vcc, 1, v152\n"
      "s_sub_u32 s48, s48, 1\n"
      "s_cbranch_vccz Lx3ok%=\n"
      "s_sleep 1\n"
      "s_cbranch_scc0 Lx3%=\n"
      "Lx3ok%=:\n"
      "s_setprio 0\n"
      "ds_read_b128 v[64:67], v112 offset:49152\n"
      ".p2align 3\n"
      "v_mfma_f32_32x32x16_bf16 v[32:47], v[68:71], v[92:95], v[32:47]\n"
      "ds_read_b128 v[60:63], v161 offset:0\n"
      "ds_read_b128 v[68:71], v112 offset:57344\n"
      ".p2align 3\n"
      "v_mfma_f32_32x32x16_bf16 v[16:31], v[72:75], v[96:99], v[16:31]\n"
      "s_waitcnt lgkmcnt(1)\n"
      "ds_read_b128 v[72:75], v113 offset:49152\n"
      "s_add_u32 m0, s66, 0\n"
      "s_xor_b32 s49, s70, 0\n"
      "buffer_load_dwordx4 v156, s[44:47], s49 offen lds\n"
      ".p2align 3\n"
      "v_mfma_f32_32x32x16_bf16 v[32:47], v[76:79], v[96:99], v[32:47]\n"
      "ds_read_b128 v[76:79], v113 offset:57344\n"
      "buffer_load_dwordx4 v157, s[44:47], s49 offen offset:1024 lds\n"
      "s_add_u32 s49, s49, 14336\n"
      ".p2align 3\n"
      "v_mfma_f32_32x32x16_bf16 v[16:31], v[80:83], v[100:103], v[16:31]\n"
      "ds_read_b128 v[80:83], v114 offset:49152\n"
      "buffer_load_dwordx4 v156, s[44:47], s49 offen offset:2048 lds\n"
      ".p2align 3\n"
      "v_mfma_f32_32x32x16_bf16 v[32:47], v[84:87], v[100:103], v[32:47]\n"
      "ds_read_b128 v[84:87], v114 offset:57344\n"
      "buffer_load_dwordx4 v157, s[44:47], s49 offen offset:3072 lds\n"
      "s_waitcnt lgkmcnt(4)\n"
      ".p2align 3\n"
      "v_mfma_f32_32x32x16_bf16 v[16:31], v[64:67], v[48:51], v[16:31]\n"
      "s_waitcnt vmcnt(7)\n"
      "ds_read_b128 v[88:91], v158 offset:4096\n"
      "ds_read_b128 v[64:67], v115 offset:49152\n"
      ".p2align 3\n"
      "v_mfma_f32_32x32x16_bf16 v[32:47], v[68:71], v[48:51], v[32:47]\n"
      "ds_read_b128 v[92:95], v159 offset:4096\n"
      "ds_read_b128 v[68:71], v115 offset:57344\n"
      "s_waitcnt lgkmcnt(6)\n"
      ".p2align 3\n"
      "v_mfma_f32_32x32x16_bf16 v[16:31], v[72:75], v[52:55], v[16:31]\n"
      "ds_read_b128 v[96:99], v160 offset:4096\n"
      "ds_read_b128 v[72:75], v116 offset:49152\n"
      ".p2align 3\n"
      "v_mfma_f32_32x32x16_bf16 v[32:47], v[76:79], v[52:55], v[32:47]\n"
      "ds_read_b128 v[100:103], v161 offset:4096\n"
      "ds_read_b128 v[76:79], v116 offset:57344\n"
      "s_waitcnt lgkmcnt(8)\n"
      ".p2align 3\n"
      "v_mfma_f32_32x32x16_bf16 v[16:31], v[80:83], v[56:59], v[16:31]\n"
      "s_waitcnt lgkmcnt(1)\n"
      "ds_read_b128 v[80:83], v117 offset:49152\n"
      "s_add_u32 m0, s66, 4096\n"
      "s_xor_b32 s49, s70, 32768\n"
      "buffer_load_dwordx4 v156, s[44:47], s49 offen lds\n"
      ".p2align 3\n"
      "v_mfma_f32_32x32x16_bf16 v[32:47], v[84:87], v[56:59], v[32:47]\n"
      "ds_read_b128 v[84:87], v117 offset:57344\n"
      "buffer_load_dwordx4 v157, s[44:47], s49 offen offset:1024 lds\n"
      "s_add_u32 s49, s49, 14336\n"
      ".p2align 3\n"
      "v_mfma_f32_32x32x16_bf16 v[16:31], v[64:67], v[60:63], v[16:31]\n"
      "ds_read_b128 v[64:67], v118 offset:49152\n"
      "buffer_load_dwordx4 v156, s[44:47], s49 offen offset:2048 lds\n"
      ".p2align 3\n"
      "v_mfma_f32_32x32x16_bf16 v[32:47], v[68:71], v[60:63], v[32:47]\n"
      "ds_read_b128 v[68:71], v118 offset:57344\n"
      "buffer_load_dwordx4 v157, s[44:47], s49 offen offset:3072 lds\n"
      "s_waitcnt lgkmcnt(4)\n"
      ".p2align 3\n"
      "v_mfma_f32_32x32x16_bf16 v[16:31], v[72:75], v[88:91], v[16:31]\n"
      "ds_read_b128 v[72:75], v119 offset:49152\n"
      ".p2align 3\n"
      "v_mfma_f32_32x32x16_bf16 v[32:47], v[76:79], v[88:91], v[32:47]\n"
      "ds_read_b128 v[76:79], v119 offset:57344\n"
      "s_waitcnt lgkmcnt(4)\n"
      ".p2align 3\n"
      "v_mfma_f32_32x32x16_bf16 v[16:31], v[80:83], v[92:95], v[16:31]\n"
      "ds_read_b128 v[80:83], v140\n"
      ".p2align 3\n"
      "v_mfma_f32_32x32x16_bf16 v[32:47], v[84:87], v[92:95], v[32:47]\n"
      "ds_read_b128 v[84:87], v141\n"
      "s_waitcnt lgkmcnt(4)\n"
      ".p2align 3\n"
      "v_mfma_f32_32x32x16_bf16 v[16:31], v[64:67], v[96:99], v[16:31]\n"
      "s_mov_b64 exec, 1\n"
      "ds_add_u32 v154, v155 offset:112\n"
      "s_mov_b64 exec, -1\n"
      ".p2align 3\n"
      "v_mfma_f32_32x32x16_bf16 v[32:47], v[68:71], v[96:99], v[32:47]\n"
      "s_waitcnt lgkmcnt(3)\n"
      ".p2align 3\n"
      "v_mfma_f32_32x32x16_bf16 v[16:31], v[72:75], v[100:103], v[16:31]\n"
      "v_mfma_f32_32x32x16_bf16 v[32:47], v[76:79], v[100:103], v[32:47]\n"
      "s_waitcnt lgkmcnt(1)\n"
      ".p2align 3\n"
      "v_mfma_f32_32x32x16_bf16 v[0:15], v[120:123], v[80:83], 0\n"
      "v_mfma_f32_32x32x16_bf16 v[0:15], v[124:127], v[84:87], v[0:15]\n"
      "s_setprio 3\n"
      "s_lshl_b32 s50, s76, 12\n"
      "s_add_u32 s50, s50, 65536\n"
      "s_add_u32 s40, s20, s50\n"
      "s_addc_u32 s41, s21, 0\n"
      "s_movk_i32 s42, 0x1000\n"
      "s_lshl_b32 s50, %[tb], 7\n"
      "s_sub_u32 s50, 3968, s50\n"
      "s_mov_b64 s[32:33], s[8:9]\n"
      "s_mov_b32 s34, 0x80000\n"
      "s_mov_b32 s35, 0x20000\n"
      ".p2align 3\n"
      "v_permlane16_swap_b32_e64 v16, v17\n"
      "v_permlane16_swap_b32_e64 v18, v19\n"
      "v_pk_fma_f32 v[50:51], v[128:129], v[16:17], v[18:19] op_sel_hi:[0,1,1]\n"
      "v_permlane16_swap_b32_e64 v20, v21\n"
      "v_pk_fma_f32 v[50:51], v[128:129], v[16:17], v[50:51] op_sel:[1,1,0] op_sel_hi:[1,0,1] neg_lo:[1,0,0]\n"
      "v_pk_fma_f32 v[48:49], v[128:129], v[50:51], v[20:21] op_sel_hi:[0,1,1]\n"
      "v_permlane16_swap_b32_e64 v22, v23\n"
      "v_pk_fma_f32 v[48:49], v[128:129], v[50:51], v[48:49] op_sel:[1,1,0] op_sel_hi:[1,0,1] neg_lo:[1,0,0]\n"
      "v_pk_fma_f32 v[50:51], v[128:129], v[48:49], v[22:23] op_sel_hi:[0,1,1]\n"
      "v_permlane16_swap_b32_e64 v24, v25\n"
      "v_pk_fma_f32 v[50:51], v[128:129], v[48:49], v[50:51] op_sel:[1,1,0] op_sel_hi:[1,0,1] neg_lo:[1,0,0]\n"
      "v_pk_fma_f32 v[48:49], v[128:129], v[50:51], v[24:25] op_sel_hi:[0,1,1]\n"
      "v_permlane16_swap_b32_e64 v26, v27\n"
      "v_pk_fma_f32 v[48:49], v[128:129], v[50:51], v[48:49] op_sel:[1,1,0] op_sel_hi:[1,0,1] neg_lo:[1,0,0]\n"
      "v_pk_fma_f32 v[50:51], v[128:129], v[48:49], v[26:27] op_sel_hi:[0,1,1]\n"
      "v_permlane16_swap_b32_e64 v28, v29\n"
      "v_pk_fma_f32 v[50:51], v[128:129], v[48:49], v[50:51] op_sel:[1,1,0] op_sel_hi:[1,0,1] neg_lo:[1,0,0]\n"
      "v_pk_fma_f32 v[48:49], v[128:129], v[50:51], v[28:29] op_sel_hi:[0,1,1]\n"
      "v_permlane16_swap_b32_e64 v30, v31\n"
      "v_pk_fma_f32 v[48:49], v[128:129], v[50:51], v[48:49] op_sel:[1,1,0] op_sel_hi:[1,0,1] neg_lo:[1,0,0]\n"
      "v_pk_fma_f32 v[50:51], v[128:129], v[48:49], v[30:31] op_sel_hi:[0,1,1]\n"
      "v_permlane16_swap_b32_e64 v32, v33\n"
      "v_pk_fma_f32 v[50:51], v[128:129], v[48:49], v[50:51] op_sel:[1,1,0] op_sel_hi:[1,0,1] neg_lo:[1,0,0]\n"
      "v_pk_fma_f32 v[48:49], v[128:129], v[50:51], v[32:33] op_sel_hi:[0,1,1]\n"
      "v_permlane16_swap_b32_e64 v34, v35\n"
      "v_pk_fma_f32 v[48:49], v[128:129], v[50:51], v[48:49] op_sel:[1,1,0] op_sel_hi:[1,0,1] neg_lo:[1,0,0]\n"
      "v_pk_fma_f32 v[50:51], v[128:129], v[48:49], v[34:35] op_sel_hi:[0,1,1]\n"
      "v_permlane16_swap_b32_e64 v36, v37\n"
      "v_pk_fma_f32 v[50:51], v[128:129], v[48:49], v[50:51] op_sel:[1,1,0] op_sel_hi:[1,0,1] neg_lo:[1,0,0]\n"
      "v_pk_fma_f32 v[48:49], v[128:129], v[50:51], v[36:37] op_sel_hi:[0,1,1]\n"
      "v_permlane16_swap_b32_e64 v38, v39\n"
      "v_pk_fma_f32 v[48:49], v[128:129], v[50:51], v[48:49] op_sel:[1,1,0] op_sel_hi:[1,0,1] neg_lo:[1,0,0]\n"
      "v_pk_fma_f32 v[50:51], v[128:129], v[48:49], v[38:39] op_sel_hi:[0,1,1]\n"
      "v_permlane16_swap_b32_e64 v40, v41\n"
      "v_pk_fma_f32 v[50:51], v[128:129], v[48:49], v[50:51] op_sel:[1,1,0] op_sel_hi:[1,0,1] neg_lo:[1,0,0]\n"
      "v_pk_fma_f32 v[48:49], v[128:129], v[50:51], v[40:41] op_sel_hi:[0,1,1]\n"
      "v_permlane16_swap_b32_e64 v42, v43\n"
      "v_pk_fma_f32 v[48:49], v[128:129], v[50:51], v[48:49] op_sel:[1,1,0] op_sel_hi:[1,0,1] neg_lo:[1,0,0]\n"
      "v_pk_fma_f32 v[50:51], v[128:129], v[48:49], v[42:43] op_sel_hi:[0,1,1]\n"
      "v_permlane16_swap_b32_e64 v44, v45\n"
      "v_pk_fma_f32 v[50:51], v[128:129], v[48:49], v[50:51] op_sel:[1,1,0] op_sel_hi:[1,0,1] neg_lo:[1,0,0]\n"
      "v_pk_fma_f32 v[48:49], v[128:129], v[50:51], v[44:45] op_sel_hi:[0,1,1]\n"
      "v_permlane16_swap_b32_e64 v46, v47\n"
      "v_pk_fma_f32 v[48:49], v[128:129], v[50:51], v[48:49] op_sel:[1,1,0] op_sel_hi:[1,0,1] neg_lo:[1,0,0]\n"
      "v_pk_fma_f32 v[50:51], v[128:129], v[48:49], v[46:47] op_sel_hi:[0,1,1]\n"
      "v_pk_fma_f32 v[50:51], v[128:129], v[48:49], v[50:51] op_sel:[1,1,0] op_sel_hi:[1,0,1] neg_lo:[1,0,0]\n"
      "ds_bpermute_b32 v52, v163, v50 offset:0\n"
      "ds_bpermute_b32 v53, v163, v51 offset:0\n"
      "ds_bpermute_b32 v54, v163, v50 offset:64\n"
      "ds_bpermute_b32 v55, v163, v51 offset:64\n"
      "ds_bpermute_b32 v56, v163, v50 offset:128\n"
      "ds_bpermute_b32 v57, v163, v51 offset:128\n"
      "ds_bpermute_b32 v58, v163, v50 offset:192\n"
      "ds_bpermute_b32 v59, v163, v51 offset:192\n"
      "ds_read_b32 v152, v154 offset:112\n"
      "s_waitcnt lgkmcnt(5)\n"
      ".p2align 3\n"
      "v_pk_fma_f32 v[60:61], v[132:133], v[52:53], v[54:55] op_sel_hi:[0,1,1]\n"
      "v_pk_fma_f32 v[60:61], v[132:133], v[52:53], v[60:61] op_sel:[1,1,0] op_sel_hi:[1,0,1] neg_lo:[1,0,0]\n"
      "v_cndmask_b32 v80, 0, v52, s[60:61]\n"
      "v_cndmask_b32 v81, 0, v53, s[60:61]\n"
      "s_waitcnt lgkmcnt(3)\n"
      ".p2align 3\n"
      "v_pk_fma_f32 v[62:63], v[132:133], v[60:61], v[56:57] op_sel_hi:[0,1,1]\n"
      "v_pk_fma_f32 v[62:63], v[132:133], v[60:61], v[62:63] op_sel:[1,1,0] op_sel_hi:[1,0,1] neg_lo:[1,0,0]\n"
      "v_cndmask_b32 v80, v80, v60, s[62:63]\n"
      "v_cndmask_b32 v81, v81, v61, s[62:63]\n"
      "s_waitcnt lgkmcnt(0)\n"
      ".p2align 3\n"
      "v_pk_fma_f32 v[48:49], v[132:133], v[62:63], v[58:59] op_sel_hi:[0,1,1]\n"
      "v_pk_fma_f32 v[48:49], v[132:133], v[62:63], v[48:49] op_sel:[1,1,0] op_sel_hi:[1,0,1] neg_lo:[1,0,0]\n"
      "v_cmp_gt_u32 vcc, 4, v152\n"
      "v_min_u32 v48, -2, v48\n"
      "v_min_u32 v49, -2, v49\n"
      "buffer_store_dwordx2 v[48:49], v149, s[40:43], s50 offen sc1\n"
      "v_cndmask_b32 v80, v80, v62, s[64:65]\n"
      "v_cndmask_b32 v81, v81, v63, s[64:65]\n"
      "s_cbranch_vccz Lsbok%=\n"
      "s_mov_b32 s48, 200000\n"
      "Lsb%=:\n"
      "ds_read_b32 v152, v154 offset:112\n"
      "s_waitcnt lgkmcnt(0)\n"
      "v_cmp_gt_u32 vcc, 4, v152\n"
      "s_sub_u32 s48, s48, 1\n"
      "s_cbranch_vccz Lsbok%=\n"
      "s_sleep 1\n"
      "s_cbranch_scc0 Lsb%=\n"
      "Lsbok%=:\n"
      "s_add_u32 m0, s67, 0\n"
      "s_add_u32 s54, s72, 0\n"
      "buffer_load_dwordx4 v162, s[32:35], s54 offen lds\n"
      "s_sleep 2\n"
      "s_add_u32 m0, s67, 1024\n"
      "s_add_u32 s54, s72, 8192\n"
      "buffer_load_dwordx4 v162, s[32:35], s54 offen lds\n"
      "s_sleep 2\n"
      "s_add_u32 m0, s67, 2048\n"
      "s_add_u32 s54, s72, 16384\n"
      "buffer_load_dwordx4 v162, s[32:35], s54 offen lds\n"
      "s_sleep 2\n"
      "s_add_u32 m0, s67, 3072\n"
      "s_add_u32 s54, s72, 24576\n"
      "buffer_load_dwordx4 v162, s[32:35], s54 offen lds\n"
      "s_sleep 2\n"
      "s_add_u32 m0, s67, 4096\n"
      "s_add_u32 s54, s72, 32768\n"
      "buffer_load_dwordx4 v162, s[32:35], s54 offen lds\n"
      "s_sleep 2\n"
      "s_add_u32 m0, s67, 5120\n"
      "s_add_u32 s54, s72, 40960\n"
      "buffer_load_dwordx4 v162, s[32:35], s54 offen lds\n"
      "s_sleep 2\n"
      "s_add_u32 m0, s67, 6144\n"
      "s_add_u32 s54, s72, 49152\n"
      "buffer_load_dwordx4 v162, s[32:35], s54 offen lds\n"
      "s_sleep 2\n"
      "s_add_u32 m0, s67, 7168\n"
      "s_add_u32 s54, s72, 57344\n"
      "buffer_load_dwordx4 v162, s[32:35], s54 offen lds\n"
      "s_sleep 2\n"
      "s_waitcnt vmcnt(8)\n"
      "ds_read_b64 v[82:83], v154 offset:48\n"
      "s_mov_b32 s48, 200000\n"
      "Le%=:\n"
      "buffer_load_dwordx2 v[64:65], v148, s[40:43], 0 offen offset:0 sc1\n"
      "buffer_load_dwordx2 v[66:67], v148, s[40:43], 0 offen offset:128 sc1\n"
      "buffer_load_dwordx2 v[68:69], v148, s[40:43], 0 offen offset:256 sc1\n"
      "buffer_load_dwordx2 v[70:71], v148, s[40:43], 0 offen offset:384 sc1\n"
      "buffer_load_dwordx2 v[72:73], v148, s[40:43], 0 offen offset:512 sc1\n"
      "buffer_load_dwordx2 v[74:75], v148, s[40:43], 0 offen offset:640 sc1\n"
      "buffer_load_dwordx2 v[76:77], v148, s[40:43], 0 offen offset:768 sc1\n"
      "buffer_load_dwordx2 v[78:79], v148, s[40:43], 0 offen offset:896 sc1\n"
      "s_waitcnt vmcnt(0)\n"
      ".p2align 3\n"
      "v_max3_u32 v48, v64, v65, v66\n"
      "v_max3_u32 v49, v67, v68, v69\n"
      "v_max3_u32 v50, v70, v71, v72\n"
      "v_max3_u32 v51, v73, v74, v75\n"
      "v_max3_u32 v48, v48, v76, v77\n"
      "v_max3_u32 v49, v49, v78, v79\n"
      "v_max3_u32 v48, v48, v50, v51\n"
      "v_max_u32 v48, v48, v49\n"
      "v_cmp_eq_u32 vcc, -1, v48\n"
      "s_sub_u32 s48, s48, 1\n"
      "s_cbranch_vccz Leok%=\n"
      "s_sleep 4\n"
      "s_cbranch_scc0 Le%=\n"
      "Leok%=:\n"
      ".p2align 3\n"
      "v_pk_fma_f32 v[76:77], v[136:137], v[78:79], v[76:77] op_sel_hi:[0,1,1]\n"
      "v_pk_fma_f32 v[76:77], v[136:137], v[78:79], v[76:77] op_sel:[1,1,0] op_sel_hi:[1,0,1] neg_lo:[1,0,0]\n"
      "v_pk_fma_f32 v[74:75], v[136:137], v[76:77], v[74:75] op_sel_hi:[0,1,1]\n"
      "v_pk_fma_f32 v[74:75], v[136:137], v[76:77], v[74:75] op_sel:[1,1,0] op_sel_hi:[1,0,1] neg_lo:[1,0,0]\n"
      "v_pk_fma_f32 v[72:73], v[136:137], v[74:75], v[72:73] op_sel_hi:[0,1,1]\n"
      "v_pk_fma_f32 v[72:73], v[136:137], v[74:75], v[72:73] op_sel:[1,1,0] op_sel_hi:[1,0,1] neg_lo:[1,0,0]\n"
      "v_pk_fma_f32 v[70:71], v[136:137], v[72:73], v[70:71] op_sel_hi:[0,1,1]\n"
      "v_pk_fma_f32 v[70:71], v[136:137], v[72:73], v[70:71] op_sel:[1,1,0] op_sel_hi:[1,0,1] neg_lo:[1,0,0]\n"
      "v_pk_fma_f32 v[68:69], v[136:137], v[70:71], v[68:69] op_sel_hi:[0,1,1]\n"
      "v_pk_fma_f32 v[68:69], v[136:137], v[70:71], v[68:69] op_sel:[1,1,0] op_sel_hi:[1,0,1] neg_lo:[1,0,0]\n"
      "v_pk_fma_f32 v[66:67], v[136:137], v[68:69], v[66:67] op_sel_hi:[0,1,1]\n"
      "v_pk_fma_f32 v[66:67], v[136:137], v[68:69], v[66:67] op_sel:[1,1,0] op_sel_hi:[1,0,1] neg_lo:[1,0,0]\n"
      "v_pk_fma_f32 v[64:65], v[136:137], v[66:67], v[64:65] op_sel_hi:[0,1,1]\n"
      "v_pk_fma_f32 v[64:65], v[136:137], v[66:67], v[64:65] op_sel:[1,1,0] op_sel_hi:[1,0,1] neg_lo:[1,0,0]\n"
      "ds_bpermute_b32 v52, v163, v64 offset:0\n"
      "ds_bpermute_b32 v53, v163, v65 offset:0\n"
      "ds_bpermute_b32 v54, v163, v64 offset:64\n"
      "ds_bpermute_b32 v55, v163, v65 offset:64\n"
      "ds_bpermute_b32 v56, v163, v64 offset:128\n"
      "ds_bpermute_b32 v57, v163, v65 offset:128\n"
      "ds_bpermute_b32 v58, v163, v64 offset:192\n"
      "ds_bpermute_b32 v59, v163, v65 offset:192\n"
      "s_waitcnt lgkmcnt(0)\n"
      ".p2align 3\n"
      "v_pk_fma_f32 v[56:57], v[138:139], v[58:59], v[56:57] op_sel_hi:[0,1,1]\n"
      "v_pk_fma_f32 v[56:57], v[138:139], v[58:59], v[56:57] op_sel:[1,1,0] op_sel_hi:[1,0,1] neg_lo:[1,0,0]\n"
      "v_pk_fma_f32 v[54:55], v[138:139], v[56:57], v[54:55] op_sel_hi:[0,1,1]\n"
      "v_pk_fma_f32 v[54:55], v[138:139], v[56:57], v[54:55] op_sel:[1,1,0] op_sel_hi:[1,0,1] neg_lo:[1,0,0]\n"
      "v_pk_fma_f32 v[52:53], v[138:139], v[54:55], v[52:53] op_sel_hi:[0,1,1]\n"
      "v_pk_fma_f32 v[52:53], v[138:139], v[54:55], v[52:53] op_sel:[1,1,0] op_sel_hi:[1,0,1] neg_lo:[1,0,0]\n"
      "v_pk_fma_f32 v[80:81], v[134:135], v[52:53], v[80:81] op_sel_hi:[0,1,1]\n"
      "v_pk_fma_f32 v[80:81], v[134:135], v[52:53], v[80:81] op_sel:[1,1,0] op_sel_hi:[1,0,1] neg_lo:[1,0,0]\n"
      "v_pk_fma_f32 v[16:17], v[128:129], v[80:81], v[16:17] op_sel_hi:[0,1,1]\n"
      "v_pk_fma_f32 v[16:17], v[128:129], v[80:81], v[16:17] op_sel:[1,1,0] op_sel_hi:[1,0,1] neg_lo:[1,0,0]\n"
      "v_pk_fma_f32 v[18:19], v[128:129], v[16:17], v[18:19] op_sel_hi:[0,1,1]\n"
      "v_pk_mul_f32 v[64:65], v[130:131], v[16:17] op_sel_hi:[0,1] neg_hi:[1,0]\n"
      "v_pk_fma_f32 v[64:65], v[130:131], v[16:17], v[64:65] op_sel:[1,1,0] op_sel_hi:[1,0,1] neg_lo:[1,0,0] neg_hi:[1,0,0]\n"
      "v_pk_fma_f32 v[18:19], v[128:129], v[16:17], v[18:19] op_sel:[1,1,0] op_sel_hi:[1,0,1] neg_lo:[1,0,0]\n"
      "v_pk_fma_f32 v[20:21], v[128:129], v[18:19], v[20:21] op_sel_hi:[0,1,1]\n"
      "v_pk_mul_f32 v[66:67], v[130:131], v[18:19] op_sel_hi:[0,1] neg_hi:[1,0]\n"
      "v_pk_fma_f32 v[66:67], v[130:131], v[18:19], v[66:67] op_sel:[1,1,0] op_sel_hi:[1,0,1] neg_lo:[1,0,0] neg_hi:[1,0,0]\n"
      "v_pk_fma_f32 v[20:21], v[128:129], v[18:19], v[20:21] op_sel:[1,1,0] op_sel_hi:[1,0,1] neg_lo:[1,0,0]\n"
      "v_cvt_pk_bf16_f32 v48, v64, v66\n"
      "v_cvt_pk_bf16_f32 v56, v65, v67\n"
      "v_pk_fma_f32 v[22:23], v[128:129], v[20:21], v[22:23] op_sel_hi:[0,1,1]\n"
      "v_pk_mul_f32 v[68:69], v[130:131], v[20:21] op_sel_hi:[0,1] neg_hi:[1,0]\n"
      "v_pk_fma_f32 v[68:69], v[130:131], v[20:21], v[68:69] op_sel:[1,1,0] op_sel_hi:[1,0,1] neg_lo:[1,0,0] neg_hi:[1,0,0]\n"
      "v_pk_fma_f32 v[22:23], v[128:129], v[20:21], v[22:23] op_sel:[1,1,0] op_sel_hi:[1,0,1] neg_lo:[1,0,0]\n"
      "v_pk_fma_f32 v[24:25], v[128:129], v[22:23], v[24:25] op_sel_hi:[0,1,1]\n"
      "v_pk_mul_f32 v[70:71], v[130:131], v[22:23] op_sel_hi:[0,1] neg_hi:[1,0]\n"
      "v_pk_fma_f32 v[70:71], v[130:131], v[22:23], v[70:71] op_sel:[1,1,0] op_sel_hi:[1,0,1] neg_lo:[1,0,0] neg_hi:[1,0,0]\n"
      "v_pk_fma_f32 v[24:25], v[128:129], v[22:23], v[24:25] op_sel:[1,1,0] op_sel_hi:[1,0,1] neg_lo:[1,0,0]\n"
      "v_cvt_pk_bf16_f32 v49, v68, v70\n"
      "v_cvt_pk_bf16_f32 v57, v69, v71\n"
      "v_pk_fma_f32 v[26:27], v[128:129], v[24:25], v[26:27] op_sel_hi:[0,1,1]\n"
      "v_pk_mul_f32 v[64:65], v[130:131], v[24:25] op_sel_hi:[0,1] neg_hi:[1,0]\n"
      "v_pk_fma_f32 v[64:65], v[130:131], v[24:25], v[64:65] op_sel:[1,1,0] op_sel_hi:[1,0,1] neg_lo:[1,0,0] neg_hi:[1,0,0]\n"
      "v_pk_fma_f32 v[26:27], v[128:129], v[24:25], v[26:27] op_sel:[1,1,0] op_sel_hi:[1,0,1] neg_lo:[1,0,0]\n"
      "v_pk_fma_f32 v[28:29], v[128:129], v[26:27], v[28:29] op_sel_hi:[0,1,1]\n"
      "v_pk_mul_f32 v[66:67], v[130:131], v[26:27] op_sel_hi:[0,1] neg_hi:[1,0]\n"
      "v_pk_fma_f32 v[66:67], v[130:131], v[26:27], v[66:67] op_sel:[1,1,0] op_sel_hi:[1,0,1] neg_lo:[1,0,0] neg_hi:[1,0,0]\n"
      "v_pk_fma_f32 v[28:29], v[128:129], v[26:27], v[28:29] op_sel:[1,1,0] op_sel_hi:[1,0,1] neg_lo:[1,0,0]\n"
      "v_cvt_pk_bf16_f32 v50, v64, v66\n"
      "v_cvt_pk_bf16_f32 v58, v65, v67\n"
      "v_pk_fma_f32 v[30:31], v[128:129], v[28:29], v[30:31] op_sel_hi:[0,1,1]\n"
      "v_pk_mul_f32 v[68:69], v[130:131], v[28:29] op_sel_hi:[0,1] neg_hi:[1,0]\n"
      "v_pk_fma_f32 v[68:69], v[130:131], v[28:29], v[68:69] op_sel:[1,1,0] op_sel_hi:[1,0,1] neg_lo:[1,0,0] neg_hi:[1,0,0]\n"
      "v_pk_fma_f32 v[30:31], v[128:129], v[28:29], v[30:31] op_sel:[1,1,0] op_sel_hi:[1,0,1] neg_lo:[1,0,0]\n"
      "v_pk_fma_f32 v[32:33], v[128:129], v[30:31], v[32:33] op_sel_hi:[0,1,1]\n"
      "v_pk_mul_f32 v[70:71], v[130:131], v[30:31] op_sel_hi:[0,1] neg_hi:[1,0]\n"
      "v_pk_fma_f32 v[70:71], v[130:131], v[30:31], v[70:71] op_sel:[1,1,0] op_sel_hi:[1,0,1] neg_lo:[1,0,0] neg_hi:[1,0,0]\n"
      "v_pk_fma_f32 v[32:33], v[128:129], v[30:31], v[32:33] op_sel:[1,1,0] op_sel_hi:[1,0,1] neg_lo:[1,0,0]\n"
      "v_cvt_pk_bf16_f32 v51, v68, v70\n"
      "v_cvt_pk_bf16_f32 v59, v69, v71\n"
      "v_pk_fma_f32 v[34:35], v[128:129], v[32:33], v[34:35] op_sel_hi:[0,1,1]\n"
      "v_pk_mul_f32 v[64:65], v[130:131], v[32:33] op_sel_hi:[0,1] neg_hi:[1,0]\n"
      "v_pk_fma_f32 v[64:65], v[130:131], v[32:33], v[64:65] op_sel:[1,1,0] op_sel_hi:[1,0,1] neg_lo:[1,0,0] neg_hi:[1,0,0]\n"
      "v_pk_fma_f32 v[34:35], v[128:129], v[32:33], v[34:35] op_sel:[1,1,0] op_sel_hi:[1,0,1] neg_lo:[1,0,0]\n"
      "v_pk_fma_f32 v[36:37], v[128:129], v[34:35], v[36:37] op_sel_hi:[0,1,1]\n"
      "v_pk_mul_f32 v[66:67], v[130:131], v[34:35] op_sel_hi:[0,1] neg_hi:[1,0]\n"
      "v_pk_fma_f32 v[66:67], v[130:131], v[34:35], v[66:67] op_sel:[1,1,0] op_sel_hi:[1,0,1] neg_lo:[1,0,0] neg_hi:[1,0,0]\n"
      "v_pk_fma_f32 v[36:37], v[128:129], v[34:35], v[36:37] op_sel:[1,1,0] op_sel_hi:[1,0,1] neg_lo:[1,0,0]\n"
      "v_cvt_pk_bf16_f32 v52, v64, v66\n"
      "v_cvt_pk_bf16_f32 v60, v65, v67\n"
      "v_pk_fma_f32 v[38:39], v[128:129], v[36:37], v[38:39] op_sel_hi:[0,1,1]\n"
      "ds_write_b128 v142, v[48:51]\n"
      "ds_write_b128 v142, v[56:59] offset:2048\n"
      ".p2align 3\n"
      "v_pk_fma_f32 v[38:39], v[128:129], v[36:37], v[38:39] op_sel:[1,1,0] op_sel_hi:[1,0,1] neg_lo:[1,0,0]\n"
      "v_pk_mul_f32 v[68:69], v[130:131], v[36:37] op_sel_hi:[0,1] neg_hi:[1,0]\n"
      "v_pk_fma_f32 v[68:69], v[130:131], v[36:37], v[68:69] op_sel:[1,1,0] op_sel_hi:[1,0,1] neg_lo:[1,0,0] neg_hi:[1,0,0]\n"
      "v_pk_fma_f32 v[40:41], v[128:129], v[38:39], v[40:41] op_sel_hi:[0,1,1]\n"
      "v_pk_mul_f32 v[70:71], v[130:131], v[38:39] op_sel_hi:[0,1] neg_hi:[1,0]\n"
      "v_pk_fma_f32 v[70:71], v[130:131], v[38:39], v[70:71] op_sel:[1,1,0] op_sel_hi:[1,0,1] neg_lo:[1,0,0] neg_hi:[1,0,0]\n"
      "v_pk_fma_f32 v[40:41], v[128:129], v[38:39], v[40:41] op_sel:[1,1,0] op_sel_hi:[1,0,1] neg_lo:[1,0,0]\n"
      "v_cvt_pk_bf16_f32 v53, v68, v70\n"
      "v_cvt_pk_bf16_f32 v61, v69, v71\n"
      "v_pk_fma_f32 v[42:43], v[128:129], v[40:41], v[42:43] op_sel_hi:[0,1,1]\n"
      "v_pk_mul_f32 v[64:65], v[130:131], v[40:41] op_sel_hi:[0,1] neg_hi:[1,0]\n"
      "v_pk_fma_f32 v[64:65], v[130:131], v[40:41], v[64:65] op_sel:[1,1,0] op_sel_hi:[1,0,1] neg_lo:[1,0,0] neg_hi:[1,0,0]\n"
      "v_pk_fma_f32 v[42:43], v[128:129], v[40:41], v[42:43] op_sel:[1,1,0] op_sel_hi:[1,0,1] neg_lo:[1,0,0]\n"
      "v_pk_fma_f32 v[44:45], v[128:129], v[42:43], v[44:45] op_sel_hi:[0,1,1]\n"
      "v_pk_mul_f32 v[66:67], v[130:131], v[42:43] op_sel_hi:[0,1] neg_hi:[1,0]\n"
      "v_pk_fma_f32 v[66:67], v[130:131], v[42:43], v[66:67] op_sel:[1,1,0] op_sel_hi:[1,0,1] neg_lo:[1,0,0] neg_hi:[1,0,0]\n"
      "v_pk_fma_f32 v[44:45], v[128:129], v[42:43], v[44:45] op_sel:[1,1,0] op_sel_hi:[1,0,1] neg_lo:[1,0,0]\n"
      "v_cvt_pk_bf16_f32 v54, v64, v66\n"
      "v_cvt_pk_bf16_f32 v62, v65, v67\n"
      "v_pk_fma_f32 v[46:47], v[128:129], v[44:45], v[46:47] op_sel_hi:[0,1,1]\n"
      "v_pk_mul_f32 v[68:69], v[130:131], v[44:45] op_sel_hi:[0,1] neg_hi:[1,0]\n"
      "v_pk_fma_f32 v[68:69], v[130:131], v[44:45], v[68:69] op_sel:[1,1,0] op_sel_hi:[1,0,1] neg_lo:[1,0,0] neg_hi:[1,0,0]\n"
      "v_pk_fma_f32 v[46:47], v[128:129], v[44:45], v[46:47] op_sel:[1,1,0] op_sel_hi:[1,0,1] neg_lo:[1,0,0]\n"
      "v_pk_mul_f32 v[70:71], v[130:131], v[46:47] op_sel_hi:[0,1] neg_hi:[1,0]\n"
      "v_pk_fma_f32 v[70:71], v[130:131], v[46:47], v[70:71] op_sel:[1,1,0] op_sel_hi:[1,0,1] neg_lo:[1,0,0] neg_hi:[1,0,0]\n"
      "v_cvt_pk_bf16_f32 v55, v68, v70\n"
      "v_cvt_pk_bf16_f32 v63, v69, v71\n"
      "ds_write_b128 v142, v[52:55] offset:16\n"
      "ds_write_b128 v142, v[60:63] offset:2064\n"
      "ds_read_b64_tr_b16 v[64:65], v143 offset:0\n"
      "ds_read_b64_tr_b16 v[66:67], v143 offset:512\n"
      "ds_read_b64_tr_b16 v[68:69], v143 offset:64\n"
      "ds_read_b64_tr_b16 v[70:71], v143 offset:576\n"
      "ds_read_b64_tr_b16 v[72:73], v143 offset:2048\n"
      "ds_read_b64_tr_b16 v[74:75], v143 offset:2560\n"
      "ds_read_b64_tr_b16 v[76:77], v143 offset:2112\n"
      "ds_read_b64_tr_b16 v[78:79], v143 offset:2624\n"
      "s_lshl_b32 s50, %[tb], 7\n"
      "s_add_u32 s50, s50, s76\n"
      "s_lshl_b32 s50, s50, 10\n"
      "s_add_u32 s50, s50, 3145728\n"
      "s_add_u32 s49, s50, 0\n"
      "s_waitcnt lgkmcnt(6)\n"
      "buffer_store_dwordx4 v[64:67], v153, s[28:31], s49 offen sc1\n"
      "s_add_u32 s49, s50, 65536\n"
      "s_waitcnt lgkmcnt(4)\n"
      "buffer_store_dwordx4 v[68:71], v153, s[28:31], s49 offen sc1\n"
      "s_add_u32 s49, s50, 32768\n"
      "s_waitcnt lgkmcnt(2)\n"
      "buffer_store_dwordx4 v[72:75], v153, s[28:31], s49 offen sc1\n"
      "s_add_u32 s49, s50, 98304\n"
      "s_waitcnt lgkmcnt(0)\n"
      "buffer_store_dwordx4 v[76:79], v153, s[28:31], s49 offen sc1\n"
      "s_add_u32 m0, s66, 8192\n"
      "s_xor_b32 s49, s70, 128\n"
      "buffer_load_dwordx4 v156, s[44:47], s49 offen lds\n"
      "buffer_load_dwordx4 v157, s[44:47], s49 offen offset:1024 lds\n"
      "s_add_u32 s49, s49, 14336\n"
      "buffer_load_dwordx4 v156, s[44:47], s49 offen offset:2048 lds\n"
      "buffer_load_dwordx4 v157, s[44:47], s49 offen offset:3072 lds\n"
      "s_lshl_b32 s50, %[tb], 8\n"
      "s_add_u32 s50, s50, 20480\n"
      "s_lshl3_add_u32 s51, s76, s50\n"
      "s_lshl_b32 s52, %[s], 6\n"
      "s_add_u32 s50, s50, s52\n"
      "s_waitcnt vmcnt(4)\n"
      "s_mov_b64 exec, 1\n"
      "buffer_store_dwordx2 v[82:83], off, s[28:31], s51 sc1\n"
      "s_mov_b64 exec, -1\n"
      "s_add_u32 m0, s67, 8192\n"
      "s_add_u32 s54, s72, 65536\n"
      "buffer_load_dwordx4 v162, s[32:35], s54 offen lds\n"
      "s_sleep 2\n"
      "s_add_u32 m0, s67, 9216\n"
      "s_add_u32 s54, s72, 73728\n"
      "buffer_load_dwordx4 v162, s[32:35], s54 offen lds\n"
      "s_sleep 2\n"
      "s_add_u32 m0, s67, 10240\n"
      "s_add_u32 s54, s72, 81920\n"
      "buffer_load_dwordx4 v162, s[32:35], s54 offen lds\n"
      "s_sleep 2\n"
      "s_add_u32 m0, s67, 11264\n"
      "s_add_u32 s54, s72, 90112\n"
      "buffer_load_dwordx4 v162, s[32:35], s54 offen lds\n"
      "s_sleep 2\n"
      "s_add_u32 m0, s67, 12288\n"
      "s_add_u32 s54, s72, 98304\n"
      "buffer_load_dwordx4 v162, s[32:35], s54 offen lds\n"
      "s_sleep 2\n"
      "s_add_u32 m0, s67, 13312\n"
      "s_add_u32 s54, s72, 106496\n"
      "buffer_load_dwordx4 v162, s[32:35], s54 offen lds\n"
      "s_sleep 2\n"
      "s_add_u32 m0, s67, 14336\n"
      "s_add_u32 s54, s72, 114688\n"
      "buffer_load_dwordx4 v162, s[32:35], s54 offen lds\n"
      "s_sleep 2\n"
      "s_add_u32 m0, s67, 15360\n"
      "s_add_u32 s54, s72, 122880\n"
      "buffer_load_dwordx4 v162, s[32:35], s54 offen lds\n"
      "s_sleep 2\n"
      "s_waitcnt vmcnt(0)\n"
      "s_mov_b32 s48, 200000\n"
      "Lh%=:\n"
      "buffer_load_dwordx2 v[64:65], v150, s[28:31], s50 offen sc1\n"
      "s_waitcnt vmcnt(0)\n"
      "v_cmp_ne_u64 vcc, v[64:65], v[82:83]\n"
      "s_sub_u32 s48, s48, 1\n"
      "s_cbranch_vccz Lhok%=\n"
      "s_sleep 4\n"
      "s_cbranch_scc0 Lh%=\n"
      "Lhok%=:\n"
      "s_add_u32 s52, s73, 0\n"
      "s_xor_b32 s53, s52, 0x10000\n"
      "buffer_load_dwordx4 v[64:67], v153, s[28:31], s52 offen offset:0 sc1\n"
      "buffer_load_dwordx4 v[68:71], v153, s[28:31], s53 offen offset:0 sc1\n"
      "buffer_load_dwordx4 v[72:75], v153, s[28:31], s52 offen offset:1024 sc1\n"
      "buffer_load_dwordx4 v[76:79], v153, s[28:31], s53 offen offset:1024 sc1\n"
      "buffer_load_dwordx4 v[80:83], v153, s[28:31], s52 offen offset:2048 sc1\n"
      "buffer_load_dwordx4 v[84:87], v153, s[28:31], s53 offen offset:2048 sc1\n"
      "buffer_load_dwordx4 v[88:91], v153, s[28:31], s52 offen offset:3072 sc1\n"
      "buffer_load_dwordx4 v[92:95], v153, s[28:31], s53 offen offset:3072 sc1\n"
      "s_mov_b64 exec, 1\n"
      "ds_add_u32 v154, v155 offset:136\n"
      "s_mov_b64 exec, -1\n"
      "s_setprio 0\n"
      "ds_read_b128 v[128:131], v158 offset:0\n"
      "ds_read_b128 v[132:135], v159 offset:0\n"
      "ds_read_b128 v[136:139], v160 offset:0\n"
      "ds_read_b128 v[140:143], v161 offset:0\n"
      "s_add_u32 s52, s73, 4096\n"
      "s_xor_b32 s53, s52, 0x10000\n"
      "buffer_load_dwordx4 v[96:99], v153, s[28:31], s52 offen offset:0 sc1\n"
      "buffer_load_dwordx4 v[100:103], v153, s[28:31], s53 offen offset:0 sc1\n"
      "buffer_load_dwordx4 v[104:107], v153, s[28:31], s52 offen offset:1024 sc1\n"
      "buffer_load_dwordx4 v[108:111], v153, s[28:31], s53 offen offset:1024 sc1\n"
      "buffer_load_dwordx4 v[112:115], v153, s[28:31], s52 offen offset:2048 sc1\n"
      "buffer_load_dwordx4 v[116:119], v153, s[28:31], s53 offen offset:2048 sc1\n"
      "buffer_load_dwordx4 v[120:123], v153, s[28:31], s52 offen offset:3072 sc1\n"
      "buffer_load_dwordx4 v[124:127], v153, s[28:31], s53 offen offset:3072 sc1\n"
      "s_waitcnt vmcnt(14)\n"
      "s_waitcnt lgkmcnt(3)\n"
      ".p2align 3\n"
      "v_mfma_f32_32x32x16_bf16 v[0:15], v[128:131], v[64:67], v[0:15]\n"
      "v_mfma_f32_32x32x16_bf16 v[16:31], v[128:131], v[68:71], 0\n"
      "ds_read_b128 v[128:131], v158 offset:4096\n"
      "s_waitcnt vmcnt(12)\n"
      "s_waitcnt lgkmcnt(3)\n"
      "s_waitcnt lgkmcnt(1)\n"
      ".p2align 3\n"
      "v_mfma_f32_32x32x16_bf16 v[0:15], v[132:135], v[72:75], v[0:15]\n"
      "s_add_u32 m0, s66, 0\n"
      "s_xor_b32 s49, s70, 32896\n"
      "buffer_load_dwordx4 v156, s[44:47], s49 offen lds\n"
      ".p2align 3\n"
      "v_mfma_f32_32x32x16_bf16 v[16:31], v[132:135], v[76:79], v[16:31]\n"
      "ds_read_b128 v[132:135], v159 offset:4096\n"
      "buffer_load_dwordx4 v157, s[44:47], s49 offen offset:1024 lds\n"
      "s_add_u32 s49, s49, 14336\n"
      "s_waitcnt vmcnt(12)\n"
      ".p2align 3\n"
      "v_mfma_f32_32x32x16_bf16 v[0:15], v[136:139], v[80:83], v[0:15]\n"
      "buffer_load_dwordx4 v156, s[44:47], s49 offen offset:2048 lds\n"
      ".p2align 3\n"
      "v_mfma_f32_32x32x16_bf16 v[16:31], v[136:139], v[84:87], v[16:31]\n"
      "ds_read_b128 v[136:139], v160 offset:4096\n"
      "buffer_load_dwordx4 v157, s[44:47], s49 offen offset:3072 lds\n"
      "s_waitcnt vmcnt(12)\n"
      ".p2align 3\n"
      "v_mfma_f32_32x32x16_bf16 v[0:15], v[140:143], v[88:91], v[0:15]\n"
      "v_mfma_f32_32x32x16_bf16 v[16:31], v[140:143], v[92:95], v[16:31]\n"
      "ds_read_b128 v[140:143], v161 offset:4096\n"
      "s_waitcnt lgkmcnt(3)\n"
      ".p2align 3\n"
      "v_mfma_f32_32x32x16_bf16 v[32:47], v[128:131], v[64:67], 0\n"
      "v_mfma_f32_32x32x16_bf16 v[48:63], v[128:131], v[68:71], 0\n"
      "ds_read_b128 v[128:131], v158 offset:8192\n"
      "s_add_u32 s52, s73, 32768\n"
      "s_xor_b32 s53, s52, 0x10000\n"
      "buffer_load_dwordx4 v[64:67], v153, s[28:31], s52 offen offset:0 sc1\n"
      "s_waitcnt lgkmcnt(3)\n"
      "s_waitcnt lgkmcnt(1)\n"
      ".p2align 3\n"
      "v_mfma_f32_32x32x16_bf16 v[32:47], v[132:135], v[72:75], v[32:47]\n"
      "s_mov_b64 s[44:45], s[18:19]\n"
      "s_add_u32 m0, s66, 4096\n"
      "s_xor_b32 s49, s70, 32768\n"
      "buffer_load_dwordx4 v156, s[44:47], s49 offen lds\n"
      ".p2align 3\n"
      "v_mfma_f32_32x32x16_bf16 v[48:63], v[132:135], v[76:79], v[48:63]\n"
      "ds_read_b128 v[132:135], v159 offset:8192\n"
      "buffer_load_dwordx4 v157, s[44:47], s49 offen offset:1024 lds\n"
      "s_add_u32 s49, s49, 14336\n"
      ".p2align 3\n"
      "v_mfma_f32_32x32x16_bf16 v[32:47], v[136:139], v[80:83], v[32:47]\n"
      "buffer_load_dwordx4 v156, s[44:47], s49 offen offset:2048 lds\n"
      ".p2align 3\n"
      "v_mfma_f32_32x32x16_bf16 v[48:63], v[136:139], v[84:87], v[48:63]\n"
      "ds_read_b128 v[136:139], v160 offset:8192\n"
      "buffer_load_dwordx4 v157, s[44:47], s49 offen offset:3072 lds\n"
      ".p2align 3\n"
      "v_mfma_f32_32x32x16_bf16 v[32:47], v[140:143], v[88:91], v[32:47]\n"
      "buffer_load_dwordx4 v[68:71], v153, s[28:31], s53 offen offset:0 sc1\n"
      ".p2align 3\n"
      "v_mfma_f32_32x32x16_bf16 v[48:63], v[140:143], v[92:95], v[48:63]\n"
      "ds_read_b128 v[140:143], v161 offset:8192\n"
      "buffer_load_dwordx4 v[72:75], v153, s[28:31], s52 offen offset:1024 sc1\n"
      "s_waitcnt vmcnt(17)\n"
      "s_waitcnt lgkmcnt(3)\n"
      ".p2align 3\n"
      "v_mfma_f32_32x32x16_bf16 v[0:15], v[128:131], v[96:99], v[0:15]\n"
      "buffer_load_dwordx4 v[76:79], v153, s[28:31], s53 offen offset:1024 sc1\n"
      ".p2align 3\n"
      "v_mfma_f32_32x32x16_bf16 v[16:31], v[128:131], v[100:103], v[16:31]\n"
      "s_waitcnt vmcnt(8)\n"
      "ds_read_b128 v[128:131], v158 offset:0\n"
      "buffer_load_dwordx4 v[80:83], v153, s[28:31], s52 offen offset:2048 sc1\n"
      "s_waitcnt lgkmcnt(3)\n"
      "s_waitcnt lgkmcnt(1)\n"
      ".p2align 3\n"
      "v_mfma_f32_32x32x16_bf16 v[0:15], v[132:135], v[104:107], v[0:15]\n"
      "s_add_u32 m0, s66, 8192\n"
      "s_xor_b32 s49, s70, 32896\n"
      "buffer_load_dwordx4 v156, s[44:47], s49 offen lds\n"
      ".p2align 3\n"
      "v_mfma_f32_32x32x16_bf16 v[16:31], v[132:135], v[108:111], v[16:31]\n"
      "ds_read_b128 v[132:135], v159 offset:0\n"
      "buffer_load_dwordx4 v157, s[44:47], s49 offen offset:1024 lds\n"
      "s_add_u32 s49, s49, 14336\n"
      ".p2align 3\n"
      "v_mfma_f32_32x32x16_bf16 v[0:15], v[136:139], v[112:115], v[0:15]\n"
      "buffer_load_dwordx4 v156, s[44:47], s49 offen offset:2048 lds\n"
      ".p2align 3\n"
      "v_mfma_f32_32x32x16_bf16 v[16:31], v[136:139], v[116:119], v[16:31]\n"
      "ds_read_b128 v[136:139], v160 offset:0\n"
      "buffer_load_dwordx4 v157, s[44:47], s49 offen offset:3072 lds\n"
      ".p2align 3\n"
      "v_mfma_f32_32x32x16_bf16 v[0:15], v[140:143], v[120:123], v[0:15]\n"
      "buffer_load_dwordx4 v[84:87], v153, s[28:31], s53 offen offset:2048 sc1\n"
      ".p2align 3\n"
      "v_mfma_f32_32x32x16_bf16 v[16:31], v[140:143], v[124:127], v[16:31]\n"
      "ds_read_b128 v[140:143], v161 offset:0\n"
      "buffer_load_dwordx4 v[88:91], v153, s[28:31], s52 offen offset:3072 sc1\n"
      "s_waitcnt lgkmcnt(3)\n"
      ".p2align 3\n"
      "v_mfma_f32_32x32x16_bf16 v[32:47], v[128:131], v[96:99], v[32:47]\n"
      "buffer_load_dwordx4 v[92:95], v153, s[28:31], s53 offen offset:3072 sc1\n"
      ".p2align 3\n"
      "v_mfma_f32_32x32x16_bf16 v[48:63], v[128:131], v[100:103], v[48:63]\n"
      "s_waitcnt vmcnt(11)\n"
      "ds_read_b128 v[128:131], v158 offset:4096\n"
      "s_add_u32 s52, s73, 36864\n"
      "s_xor_b32 s53, s52, 0x10000\n"
      "buffer_load_dwordx4 v[96:99], v153, s[28:31], s52 offen offset:0 sc1\n"
      "s_waitcnt lgkmcnt(3)\n"
      "s_waitcnt lgkmcnt(1)\n"
      ".p2align 3\n"
      "v_mfma_f32_32x32x16_bf16 v[32:47], v[132:135], v[104:107], v[32:47]\n"
      "s_add_u32 m0, s66, 0\n"
      "s_xor_b32 s49, s70, 0\n"
      "buffer_load_dwordx4 v156, s[44:47], s49 offen lds\n"
      ".p2align 3\n"
      "v_mfma_f32_32x32x16_bf16 v[48:63], v[132:135], v[108:111], v[48:63]\n"
      "ds_read_b128 v[132:135], v159 offset:4096\n"
      "buffer_load_dwordx4 v157, s[44:47], s49 offen offset:1024 lds\n"
      "s_add_u32 s49, s49, 14336\n"
      ".p2align 3\n"
      "v_mfma_f32_32x32x16_bf16 v[32:47], v[136:139], v[112:115], v[32:47]\n"
      "buffer_load_dwordx4 v156, s[44:47], s49 offen offset:2048 lds\n"
      ".p2align 3\n"
      "v_mfma_f32_32x32x16_bf16 v[48:63], v[136:139], v[116:119], v[48:63]\n"
      "ds_read_b128 v[136:139], v160 offset:4096\n"
      "buffer_load_dwordx4 v157, s[44:47], s49 offen offset:3072 lds\n"
      ".p2align 3\n"
      "v_mfma_f32_32x32x16_bf16 v[32:47], v[140:143], v[120:123], v[32:47]\n"
      "buffer_load_dwordx4 v[100:103], v153, s[28:31], s53 offen offset:0 sc1\n"
      ".p2align 3\n"
      "v_mfma_f32_32x32x16_bf16 v[48:63], v[140:143], v[124:127], v[48:63]\n"
      "ds_read_b128 v[140:143], v161 offset:4096\n"
      "buffer_load_dwordx4 v[104:107], v153, s[28:31], s52 offen offset:1024 sc1\n"
      "buffer_load_dwordx4 v[108:111], v153, s[28:31], s53 offen offset:1024 sc1\n"
      "buffer_load_dwordx4 v[112:115], v153, s[28:31], s52 offen offset:2048 sc1\n"
      "buffer_load_dwordx4 v[116:119], v153, s[28:31], s53 offen offset:2048 sc1\n"
      "buffer_load_dwordx4 v[120:123], v153, s[28:31], s52 offen offset:3072 sc1\n"
      "buffer_load_dwordx4 v[124:127], v153, s[28:31], s53 offen offset:3072 sc1\n"
      "s_waitcnt lgkmcnt(0)\n"
      "s_add_u32 m0, s66, 4096\n"
      "s_xor_b32 s49, s70, 128\n"
      "buffer_load_dwordx4 v156, s[44:47], s49 offen lds\n"
      "buffer_load_dwordx4 v157, s[44:47], s49 offen offset:1024 lds\n"
      "s_add_u32 s49, s49, 14336\n"
      "buffer_load_dwordx4 v156, s[44:47], s49 offen offset:2048 lds\n"
      "buffer_load_dwordx4 v157, s[44:47], s49 offen offset:3072 lds\n"
      "s_setprio 3\n"
      "s_mov_b64 s[32:33], s[24:25]\n"
      "s_mov_b32 s34, 0x800000\n"
      "s_mov_b32 s35, 0x20000\n"
      "s_lshl_b32 s49, %[s], 2\n"
      "v_mov_b32 v144, s49\n"
      "s_mov_b32 s48, 200000\n"
      "Lp%=:\n"
      "ds_read_b32 v152, v144 offset:176\n"
      "s_waitcnt lgkmcnt(0)\n"
      "v_cmp_gt_u32 vcc, 2, v152\n"
      "s_sub_u32 s48, s48, 1\n"
      "s_cbranch_vccz Lpok%=\n"
      "s_sleep 3\n"
      "s_cbranch_scc0 Lp%=\n"
      "Lpok%=:\n"
      "s_setprio 0\n"
      "s_waitcnt vmcnt(26)\n"
      ".p2align 3\n"
      "v_mfma_f32_32x32x16_bf16 v[32:47], v[128:131], v[64:67], v[32:47]\n"
      "v_mfma_f32_32x32x16_bf16 v[48:63], v[128:131], v[68:71], v[48:63]\n"
      "s_waitcnt vmcnt(19)\n"
      "ds_read_b128 v[128:131], v158 offset:8192\n"
      "v_add_u32 v144, s66, v153\n"
      "s_xor_b32 s48, %[s], 1\n"
      "s_mul_i32 s48, s48, 12288\n"
      "s_add_u32 s48, s48, 46080\n"
      "v_add_u32 v145, s48, v153\n"
      "s_xor_b32 s48, %[s], 2\n"
      "s_mul_i32 s48, s48, 12288\n"
      "s_add_u32 s48, s48, 46080\n"
      "v_add_u32 v146, s48, v153\n"
      "s_xor_b32 s48, %[s], 3\n"
      "s_mul_i32 s48, s48, 12288\n"
      "s_add_u32 s48, s48, 46080\n"
      "v_add_u32 v147, s48, v153\n"
      ".p2align 3\n"
      "v_mfma_f32_32x32x16_bf16 v[32:47], v[132:135], v[72:75], v[32:47]\n"
      "v_mfma_f32_32x32x16_bf16 v[48:63], v[132:135], v[76:79], v[48:63]\n"
      "ds_read_b128 v[132:135], v159 offset:8192\n"
      "v_lshrrev_b32 v152, 5, v153\n"
      "v_and_b32 v152, 16, v152\n"
      "v_xor_b32 v152, v152, v153\n"
      "v_and_b32 v152, 0x70, v152\n"
      "v_and_b32 v148, 0x1f0, v153\n"
      "v_lshl_or_b32 v148, v148, 3, v152\n"
      "s_waitcnt vmcnt(18)\n"
      ".p2align 3\n"
      "v_mfma_f32_32x32x16_bf16 v[32:47], v[136:139], v[80:83], v[32:47]\n"
      "v_mfma_f32_32x32x16_bf16 v[48:63], v[136:139], v[84:87], v[48:63]\n"
      "ds_read_b128 v[136:139], v160 offset:8192\n"
      "s_xor_b32 s48, %[s], 2\n"
      "s_mul_i32 s48, s48, 12288\n"
      "s_add_u32 s48, s48, 54272\n"
      "v_add_u32 v148, s48, v148\n"
      "v_xor_b32 v149, 32, v148\n"
      "v_xor_b32 v150, 64, v148\n"
      "v_xor_b32 v162, 96, v148\n"
      "s_waitcnt vmcnt(16)\n"
      ".p2align 3\n"
      "v_mfma_f32_32x32x16_bf16 v[32:47], v[140:143], v[88:91], v[32:47]\n"
      "v_mfma_f32_32x32x16_bf16 v[48:63], v[140:143], v[92:95], v[48:63]\n"
      "ds_read_b128 v[140:143], v161 offset:8192\n"
      "v_lshrrev_b32 v152, 7, v153\n"
      "v_lshlrev_b32 v156, 4, v152\n"
      "v_xor_b32 v156, v156, v153\n"
      "v_add_u32 v156, s48, v156\n"
      "v_and_b32 v157, 0x70, v153\n"
      "v_lshl_or_b32 v157, v152, 12, v157\n"
      "s_waitcnt vmcnt(10)\n"
      "s_waitcnt lgkmcnt(3)\n"
      ".p2align 3\n"
      "v_mfma_f32_32x32x16_bf16 v[32:47], v[128:131], v[96:99], v[32:47]\n"
      "v_mfma_f32_32x32x16_bf16 v[48:63], v[128:131], v[100:103], v[48:63]\n"
      "ds_read_b128 v[128:131], v158 offset:0\n"
      "s_waitcnt vmcnt(8)\n"
      "s_waitcnt lgkmcnt(3)\n"
      ".p2align 3\n"
      "v_mfma_f32_32x32x16_bf16 v[32:47], v[132:135], v[104:107], v[32:47]\n"
      "v_mfma_f32_32x32x16_bf16 v[48:63], v[132:135], v[108:111], v[48:63]\n"
      "ds_read_b128 v[132:135], v159 offset:0\n"
      "s_waitcnt vmcnt(6)\n"
      "s_waitcnt lgkmcnt(3)\n"
      ".p2align 3\n"
      "v_mfma_f32_32x32x16_bf16 v[32:47], v[136:139], v[112:115], v[32:47]\n"
      "v_mfma_f32_32x32x16_bf16 v[48:63], v[136:139], v[116:119], v[48:63]\n"
      "ds_read_b128 v[136:139], v160 offset:0\n"
      "s_waitcnt vmcnt(4)\n"
      "s_waitcnt lgkmcnt(3)\n"
      ".p2align 3\n"
      "v_mfma_f32_32x32x16_bf16 v[32:47], v[140:143], v[120:123], v[32:47]\n"
      "v_mfma_f32_32x32x16_bf16 v[48:63], v[140:143], v[124:127], v[48:63]\n"
      "ds_read_b128 v[140:143], v161 offset:0\n"
      "s_waitcnt lgkmcnt(3)\n"
      ".p2align 3\n"
      "v_mfma_f32_32x32x16_bf16 v[16:31], v[128:131], v[68:71], v[16:31]\n"
      "s_waitcnt vmcnt(0)\n"
      "ds_read_b128 v[128:131], v158 offset:4096\n"
      "s_waitcnt lgkmcnt(3)\n"
      ".p2align 3\n"
      "v_mfma_f32_32x32x16_bf16 v[16:31], v[132:135], v[76:79], v[16:31]\n"
      "ds_read_b128 v[132:135], v159 offset:4096\n"
      "s_waitcnt lgkmcnt(3)\n"
      ".p2align 3\n"
      "v_mfma_f32_32x32x16_bf16 v[16:31], v[136:139], v[84:87], v[16:31]\n"
      "ds_read_b128 v[136:139], v160 offset:4096\n"
      "s_waitcnt lgkmcnt(3)\n"
      ".p2align 3\n"
      "v_mfma_f32_32x32x16_bf16 v[16:31], v[140:143], v[92:95], v[16:31]\n"
      "ds_read_b128 v[140:143], v161 offset:4096\n"
      "ds_write_b128 v144, v[32:35] offset:8192\n"
      "s_waitcnt lgkmcnt(4)\n"
      ".p2align 3\n"
      "v_mfma_f32_32x32x16_bf16 v[16:31], v[128:131], v[100:103], v[16:31]\n"
      "ds_read_b128 v[128:131], v158 offset:0\n"
      "ds_write_b128 v144, v[36:39] offset:9216\n"
      "s_waitcnt lgkmcnt(5)\n"
      ".p2align 3\n"
      "v_mfma_f32_32x32x16_bf16 v[16:31], v[132:135], v[108:111], v[16:31]\n"
      "ds_read_b128 v[132:135], v159 offset:0\n"
      "ds_write_b128 v144, v[40:43] offset:10240\n"
      "s_waitcnt lgkmcnt(6)\n"
      ".p2align 3\n"
      "v_mfma_f32_32x32x16_bf16 v[16:31], v[136:139], v[116:119], v[16:31]\n"
      "ds_read_b128 v[136:139], v160 offset:0\n"
      "ds_write_b128 v144, v[44:47] offset:11264\n"
      "s_waitcnt lgkmcnt(7)\n"
      ".p2align 3\n"
      "v_mfma_f32_32x32x16_bf16 v[16:31], v[140:143], v[124:127], v[16:31]\n"
      "ds_read_b128 v[140:143], v161 offset:0\n"
      "ds_write_b128 v144, v[48:51] offset:0\n"
      "s_waitcnt lgkmcnt(7)\n"
      ".p2align 3\n"
      "v_mfma_f32_32x32x16_bf16 v[0:15], v[128:131], v[64:67], v[0:15]\n"
      "ds_read_b128 v[128:131], v158 offset:4096\n"
      "ds_write_b128 v144, v[52:55] offset:1024\n"
      "s_waitcnt lgkmcnt(7)\n"
      ".p2align 3\n"
      "v_mfma_f32_32x32x16_bf16 v[0:15], v[132:135], v[72:75], v[0:15]\n"
      "ds_read_b128 v[132:135], v159 offset:4096\n"
      "ds_write_b128 v144, v[56:59] offset:2048\n"
      "s_waitcnt lgkmcnt(7)\n"
      ".p2align 3\n"
      "v_mfma_f32_32x32x16_bf16 v[0:15], v[136:139], v[80:83], v[0:15]\n"
      "ds_read_b128 v[136:139], v160 offset:4096\n"
      "ds_write_b128 v144, v[60:63] offset:3072\n"
      "s_waitcnt lgkmcnt(7)\n"
      ".p2align 3\n"
      "v_mfma_f32_32x32x16_bf16 v[0:15], v[140:143], v[88:91], v[0:15]\n"
      "ds_read_b128 v[140:143], v161 offset:4096\n"
      "ds_write_b128 v144, v[16:19] offset:4096\n"
      "ds_write_b128 v144, v[20:23] offset:5120\n"
      "s_waitcnt lgkmcnt(8)\n"
      ".p2align 3\n"
      "v_mfma_f32_32x32x16_bf16 v[0:15], v[128:131], v[96:99], v[0:15]\n"
      "ds_write_b128 v144, v[24:27] offset:6144\n"
      "ds_write_b128 v144, v[28:31] offset:7168\n"
      "s_mov_b64 exec, 1\n"
      "ds_add_u32 v154, v155 offset:112\n"
      "s_mov_b64 exec, -1\n"
      "s_waitcnt lgkmcnt(9)\n"
      ".p2align 3\n"
      "v_mfma_f32_32x32x16_bf16 v[0:15], v[132:135], v[104:107], v[0:15]\n"
      "s_waitcnt lgkmcnt(7)\n"
      ".p2align 3\n"
      "v_mfma_f32_32x32x16_bf16 v[0:15], v[136:139], v[112:115], v[0:15]\n"
      "s_waitcnt lgkmcnt(5)\n"
      ".p2align 3\n"
      "v_mfma_f32_32x32x16_bf16 v[0:15], v[140:143], v[120:123], v[0:15]\n"
      "s_setprio 3\n"
      "s_mov_b32 s48, 200000\n"
      "Lb%=:\n"
      "ds_read_b32 v152, v154 offset:112\n"
      "s_waitcnt lgkmcnt(0)\n"
      "v_cmp_gt_u32 vcc, 8, v152\n"
      "s_sub_u32 s48, s48, 1\n"
      "s_cbranch_vccz Lbok%=\n"
      "s_cbranch_scc0 Lb%=\n"
      "Lbok%=:\n"
      "ds_read_b128 v[16:19], v145 offset:4096\n"
      "ds_read_b128 v[32:35], v146 offset:8192\n"
      "ds_read_b128 v[48:51], v147 offset:0\n"
      "ds_read_b128 v[20:23], v145 offset:5120\n"
      "ds_read_b128 v[36:39], v146 offset:9216\n"
      "ds_read_b128 v[52:55], v147 offset:1024\n"
      "ds_read_b128 v[24:27], v145 offset:6144\n"
      "ds_read_b128 v[40:43], v146 offset:10240\n"
      "ds_read_b128 v[56:59], v147 offset:2048\n"
      "ds_read_b128 v[28:31], v145 offset:7168\n"
      "ds_read_b128 v[44:47], v146 offset:11264\n"
      "ds_read_b128 v[60:63], v147 offset:3072\n"
      "s_waitcnt lgkmcnt(9)\n"
      ".p2align 3\n"
      "v_pk_add_f32 v[0:1], v[0:1], v[16:17]\n"
      "v_pk_add_f32 v[32:33], v[32:33], v[48:49]\n"
      "v_pk_add_f32 v[2:3], v[2:3], v[18:19]\n"
      "v_pk_add_f32 v[34:35], v[34:35], v[50:51]\n"
      "v_pk_add_f32 v[0:1], v[0:1], v[32:33]\n"
      "v_pk_add_f32 v[2:3], v[2:3], v[34:35]\n"
      "ds_write_b128 v148, v[0:3]\n"
      "s_waitcnt lgkmcnt(7)\n"
      ".p2align 3\n"
      "v_pk_add_f32 v[4:5], v[4:5], v[20:21]\n"
      "v_pk_add_f32 v[36:37], v[36:37], v[52:53]\n"
      "v_pk_add_f32 v[6:7], v[6:7], v[22:23]\n"
      "v_pk_add_f32 v[38:39], v[38:39], v[54:55]\n"
      "v_pk_add_f32 v[4:5], v[4:5], v[36:37]\n"
      "v_pk_add_f32 v[6:7], v[6:7], v[38:39]\n"
      "ds_write_b128 v149, v[4:7]\n"
      "s_waitcnt lgkmcnt(5)\n"
      ".p2align 3\n"
      "v_pk_add_f32 v[8:9], v[8:9], v[24:25]\n"
      "v_pk_add_f32 v[40:41], v[40:41], v[56:57]\n"
      "v_pk_add_f32 v[10:11], v[10:11], v[26:27]\n"
      "v_pk_add_f32 v[42:43], v[42:43], v[58:59]\n"
      "v_pk_add_f32 v[8:9], v[8:9], v[40:41]\n"
      "v_pk_add_f32 v[10:11], v[10:11], v[42:43]\n"
      "ds_write_b128 v150, v[8:11]\n"
      "s_waitcnt lgkmcnt(3)\n"
      ".p2align 3\n"
      "v_pk_add_f32 v[12:13], v[12:13], v[28:29]\n"
      "v_pk_add_f32 v[44:45], v[44:45], v[60:61]\n"
      "v_pk_add_f32 v[14:15], v[14:15], v[30:31]\n"
      "v_pk_add_f32 v[46:47], v[46:47], v[62:63]\n"
      "v_pk_add_f32 v[12:13], v[12:13], v[44:45]\n"
      "v_pk_add_f32 v[14:15], v[14:15], v[46:47]\n"
      "ds_write_b128 v162, v[12:15]\n"
      "ds_read_b128 v[16:19], v156 offset:0\n"
      "ds_read_b128 v[20:23], v156 offset:1024\n"
      "ds_read_b128 v[24:27], v156 offset:2048\n"
      "ds_read_b128 v[28:31], v156 offset:3072\n"
      "s_waitcnt lgkmcnt(3)\n"
      "buffer_store_dwordx4 v[16:19], v157, s[32:35], s75 offen sc1\n"
      "s_add_u32 s75, s75, 0x8000\n"
      "s_waitcnt lgkmcnt(2)\n"
      "buffer_store_dwordx4 v[20:23], v157, s[32:35], s75 offen sc1\n"
      "s_add_u32 s75, s75, 0x8000\n"
      "s_waitcnt lgkmcnt(1)\n"
      "buffer_store_dwordx4 v[24:27], v157, s[32:35], s75 offen sc1\n"
      "s_add_u32 s75, s75, 0x8000\n"
      "s_waitcnt lgkmcnt(0)\n"
      "buffer_store_dwordx4 v[28:31], v157, s[32:35], s75 offen sc1\n"
      "s_cmp_eq_u32 %[bid], 199\n"
      "s_cselect_b32 s48, %[s], 1\n"
      "s_cmp_lg_u32 s48, 0\n"
      "s_cbranch_scc1 Lnb%=\n"
      "ds_read_b64 v[0:1], v154 offset:56\n"
      "s_mov_b64 exec, 1\n"
      "s_waitcnt lgkmcnt(0)\n"
      "buffer_store_dwordx2 v[0:1], off, s[28:31], 0 sc1\n"
      "s_mov_b64 exec, -1\n"
      "Lnb%=:\n"
      "Lend%=:\n"
      "s_endpgm\n"
      : "+{v[112:119]}"(xa), "+{v[120:127]}"(dg), "+{v[128:131]}"(k0), "+{v[132:135]}"(k1), "+{v[136:139]}"(k2), "+{v[140:143]}"(m1), "+{v[144:151]}"(m0), "+{v[152:167]}"(ps)
      : [ka] "s"(ka), [s] "s"(s), [tb] "s"(tb), [j] "s"(j), [bid] "s"(b)
      : "v0", "v1", "v2", "v3", "v4", "v5", "v6", "v7", "v8", "v9", "v10", "v11", "v12", "v13", "v14", "v15", "v16", "v17", "v18", "v19", "v20", "v21", "v22", "v23", "v24", "v25", "v26", "v27", "v28", "v29", "v30", "v31", "v32", "v33", "v34", "v35", "v36", "v37", "v38", "v39", "v40", "v41", "v42", "v43", "v44", "v45", "v46", "v47", "v48", "v49", "v50", "v51", "v52", "v53", "v54", "v55", "v56", "v57", "v58", "v59", "v60", "v61", "v62", "v63", "v64", "v65", "v66", "v67", "v68", "v69", "v70", "v71", "v72", "v73", "v74", "v75", "v76", "v77", "v78", "v79", "v80", "v81", "v82", "v83", "v84", "v85", "v86", "v87", "v88", "v89", "v90", "v91", "v92", "v93", "v94", "v95", "v96", "v97", "v98", "v99", "v100", "v101", "v102", "v103", "v104", "v105", "v106", "v107", "v108", "v109", "v110", "v111", "s8", "s9", "s10", "s11", "s12", "s13", "s14", "s15", "s16", "s17", "s18", "s19", "s20", "s21", "s22", "s23", "s24", "s25", "s26", "s27", "s28", "s29", "s30", "s31", "s32", "s33", "s34", "s35", "s36", "s37", "s38", "s39", "s40", "s41", "s42", "s43", "s44", "s45", "s46", "s47", "s48", "s49", "s50", "s51", "s52", "s53", "s54", "s55", "s56", "s57", "s58", "s59", "s60", "s61", "s62", "s63", "s64", "s65", "s66", "s67", "s68", "s69", "s70", "s71", "s72", "s73", "s74", "s75", "s76", "s77", "s78", "vcc", "scc", "memory");
}

extern "C" void kernel_launch(void* const* d_in, const int* in_sizes, int n_in, void* d_out, int out_size, void* d_ws, size_t ws_size, hipStream_t stream) {
  Params p;
  p.xa = Desc{d_in[0], 2048 * 1024, 0x20000}; p.wq = Desc{d_in[1], 512 * 1024, 0x20000};
  p.wk = Desc{d_in[2], 16384, 0x20000}; p.wv = Desc{d_in[3], 16384, 0x20000};
  p.ws = Desc{d_ws, W_END, 0x20000}; p.out = Desc{d_out, 2048 * 4096, 0x20000};
  p.wo = d_in[4]; p.xb = d_in[5]; p.lre = d_in[6]; p.lim = d_in[7]; p.bre = d_in[8]; p.bim = d_in[9];
  p.cre = d_in[10]; p.cim = d_in[11]; p.ldt = d_in[12]; p.dsk = d_in[13];
  fused_kernel<<<256, 768, 0, stream>>>(p);
}
```
